```python
import torch

def _softmax(x, axis=-1):
    return torch.softmax(x, dim=axis)
def _gelu(x):
    return torch.nn.functional.gelu(x, approximate="tanh")
def _silu(x):
    return torch.nn.functional.silu(x)
def _sigmoid(x):
    return torch.sigmoid(x)
def _layernorm(x, axis=-1, eps=1e-5):
    m = x.mean(dim=axis, keepdim=True)
    v = x.var(dim=axis, keepdim=True, unbiased=False)
    return (x - m) / torch.sqrt(v + eps)
def _scatter_add(tgt, idx, src, axis=0):
    return tgt.clone().index_add_(axis, idx, src)
def _segment_sum(src, seg, n):
    out = torch.zeros((n,) + src.shape[1:], dtype=src.dtype)
    return out.index_add_(0, seg, src)
def _pad(x, pw):
    flat = tuple(v for pair in reversed(pw) for v in pair)
    return torch.nn.functional.pad(x, flat)

kernel_name = 'standalone__swiglu_ffn__prompt'

def setup_inputs():

    _w = (lambda a: (lambda t: t.to(torch.bfloat16) if t.is_floating_point() else t)(torch.as_tensor(a)))
    B, S, D, F = 1, 2048, 512, 1376
    return {
        "x": _w((lambda *s: torch.randn(*s, dtype=torch.bfloat16) * 0.05)(B, S, D)),
        "w_gate": _w((lambda *s: torch.randn(*s, dtype=torch.bfloat16) * 0.05)(D, F)),
        "w_up": _w((lambda *s: torch.randn(*s, dtype=torch.bfloat16) * 0.05)(D, F)),
        "w_down": _w((lambda *s: torch.randn(*s, dtype=torch.bfloat16) * 0.05)(F, D)),
    }

def reference(x, w_gate, w_up, w_down):
    g = _silu(x @ w_gate)
    u = x @ w_up
    return (g * u) @ w_down
```

```cpp
#include <hip/hip_runtime.h>
#include <hip/hip_bf16.h>

typedef short s16x8 __attribute__((ext_vector_type(8)));
typedef short s16x4 __attribute__((ext_vector_type(4)));
typedef __bf16 bf16x8 __attribute__((ext_vector_type(8)));
typedef float f32x4 __attribute__((ext_vector_type(4)));
typedef float f32x16 __attribute__((ext_vector_type(16)));
typedef unsigned u32x4 __attribute__((ext_vector_type(4)));
typedef unsigned u32x2 __attribute__((ext_vector_type(2)));
#define LAS __attribute__((address_space(3)))

constexpr int M_ = 2048, D_ = 512, F_ = 1376;
constexpr int NS2 = F_ / 32;

__device__ __forceinline__ unsigned pack_bf16(float lo, float hi) {
  typedef __bf16 bf16x2 __attribute__((ext_vector_type(2)));
  typedef float f32x2 __attribute__((ext_vector_type(2)));
  f32x2 v = {lo, hi};
  bf16x2 b = __builtin_convertvector(v, bf16x2);
  return __builtin_bit_cast(unsigned, b);
}
__device__ __forceinline__ float bf_lo(unsigned p) { return __builtin_bit_cast(float, p << 16); }
__device__ __forceinline__ float bf_hi(unsigned p) { return __builtin_bit_cast(float, p & 0xffff0000u); }

__device__ __forceinline__ float silu_f(float g) {
  float e = __builtin_amdgcn_exp2f(g * -1.4426950408889634f);
  return g * __builtin_amdgcn_rcpf(1.0f + e);
}

constexpr int STG_X = 16384, STG_W = 12288, STG = STG_X + 2 * STG_W;
constexpr int NSLOT = 4;

__global__ __launch_bounds__(256, 1) void k1_gate_up(const unsigned short* __restrict__ x,
                                                      const unsigned short* __restrict__ wg,
                                                      const unsigned short* __restrict__ wu,
                                                      unsigned char* __restrict__ hws) {
  extern __shared__ __attribute__((aligned(16))) unsigned char smem[];
  const int tid = threadIdx.x, lane = tid & 63;
  const int w = __builtin_amdgcn_readfirstlane(tid >> 6);
  const int b = blockIdx.x;
  const int xcd = b & 7, idx = b >> 3;
  int mt, ft;
  {
    const int lin = (xcd >> 1) * 60 + (xcd & 1) * 30 + idx;
    const int pair = xcd >> 1, mh = xcd & 1;
    const int l2 = pair * 30 + idx;
    ft = l2 >> 3;
    mt = mh * 8 + (l2 & 7);
    (void)lin;
  }
  const int m0 = mt * 128, f0 = ft * 96;
  const int wm = w >> 1, wf = w & 1;

  __amdgpu_buffer_rsrc_t rx = __builtin_amdgcn_make_buffer_rsrc((void*)x, 0, M_ * D_ * 2, 0x00020000);
  __amdgpu_buffer_rsrc_t rg = __builtin_amdgcn_make_buffer_rsrc((void*)wg, 0, D_ * F_ * 2, 0x00020000);
  __amdgpu_buffer_rsrc_t ru = __builtin_amdgcn_make_buffer_rsrc((void*)wu, 0, D_ * F_ * 2, 0x00020000);

  int xoff[4];
#pragma unroll
  for (int i = 0; i < 4; ++i) {
    const int p = w * 4 + i;
    const int row = p * 8 + (lane >> 3);
    const int ch = (lane & 7) ^ ((row >> 1) & 7);
    xoff[i] = (m0 + row) * (D_ * 2) + ch * 16;
  }
  int goff[3], uoff[3];
#pragma unroll
  for (int i = 0; i < 3; ++i) {
    const int c = (w * 3 + i) * 64 + lane;
    const int k = c / 12, cc = c % 12;
    const int seg = cc >> 1, half = cc & 1;
    int colg = f0 + seg * 16 + half * 8;
    int colu = f0 + (seg ^ 1) * 16 + half * 8;
    colg = colg < F_ ? colg : F_ - 8;
    colu = colu < F_ ? colu : F_ - 8;
    goff[i] = k * (F_ * 2) + colg * 2;
    uoff[i] = k * (F_ * 2) + colu * 2;
  }

  const unsigned ldsb = (unsigned)(size_t)(LAS unsigned char*)smem;
  const unsigned m0x = ldsb + w * 4096, m0g = ldsb + STG_X + w * 3072, m0u = m0g + STG_W;
#define GLDS(M0V, VOFF, RS, SOFF)                                                                      \
  asm volatile("s_mov_b32 m0, %0\n\ts_nop 0\n\tbuffer_load_dwordx4 %1, %2, %3 offen lds" ::"s"(M0V), \
               "v"(VOFF), "s"(RS), "s"(SOFF)                                                           \
               : "memory")
  auto issue = [&](int s) {
    const unsigned sb = (s & (NSLOT - 1)) * STG;
#pragma unroll
    for (int i = 0; i < 4; ++i) GLDS(m0x + sb + i * 1024, xoff[i], rx, s * 128);
#pragma unroll
    for (int i = 0; i < 3; ++i) GLDS(m0g + sb + i * 1024, goff[i], rg, s * 64 * F_ * 2);
#pragma unroll
    for (int i = 0; i < 3; ++i) GLDS(m0u + sb + i * 1024, uoff[i], ru, s * 64 * F_ * 2);
  };

  int xrd[2];
#pragma unroll
  for (int t = 0; t < 2; ++t) {
    const int row = wm * 64 + t * 32 + (lane & 31);
    xrd[t] = row * 128;
  }
  const int xsw = ((lane & 31) >> 1) & 7;
  const int hh = lane >> 5;
  const int bsel = (lane >> 4) & 1;
  const int wrow = 8 * hh + ((lane >> 2) & 3);
  const int wbase = STG_X + bsel * STG_W + wrow * 192 + (lane & 3) * 8;

  f32x16 acc[2][3];
#pragma unroll
  for (int a = 0; a < 2; ++a)
#pragma unroll
    for (int u = 0; u < 3; ++u)
#pragma unroll
      for (int r = 0; r < 16; ++r) acc[a][u][r] = 0.f;

  issue(0);
  issue(1);
  issue(2);
  issue(3);

  s16x8 bx[2][2], aw[2][3];
  auto rdfrag = [&](int g, s16x8 (&b_)[2], s16x8 (&a_)[3]) {
    const int s = g >> 2, ks = g & 3;
    const LAS unsigned char* base = (const LAS unsigned char*)smem + (s & (NSLOT - 1)) * STG;
#pragma unroll
    for (int u = 0; u < 3; ++u) {
      const int seg = (wf * 3 + u) ^ bsel;
      const LAS unsigned char* p = base + wbase + ks * 16 * 192 + seg * 32;
      s16x4 lo = __builtin_amdgcn_ds_read_tr16_b64_v4i16((LAS s16x4*)p);
      s16x4 hi = __builtin_amdgcn_ds_read_tr16_b64_v4i16((LAS s16x4*)(p + 4 * 192));
      a_[u] = __builtin_shufflevector(lo, hi, 0, 1, 2, 3, 4, 5, 6, 7);
    }
#pragma unroll
    for (int t = 0; t < 2; ++t) b_[t] = *(const LAS s16x8*)(base + xrd[t] + (((2 * ks + hh) ^ xsw) * 16));
  };
  asm volatile("s_waitcnt vmcnt(30)" ::: "memory");
  __builtin_amdgcn_s_barrier();
  asm volatile("" ::: "memory");
  rdfrag(0, bx[0], aw[0]);
#pragma unroll
  for (int g = 0; g < 32; ++g) {
    const int s = g >> 2;
    if ((g & 3) == 3 && s < 7) {
      __builtin_amdgcn_sched_barrier(0);
      if (s <= 4) asm volatile("s_waitcnt vmcnt(20) lgkmcnt(0)" ::: "memory");
      else if (s == 5) asm volatile("s_waitcnt vmcnt(10) lgkmcnt(0)" ::: "memory");
      else asm volatile("s_waitcnt vmcnt(0) lgkmcnt(0)" ::: "memory");
      __builtin_amdgcn_s_barrier();
      asm volatile("" ::: "memory");
    }
    __builtin_amdgcn_sched_barrier(0);
    if (g + 1 < 32) rdfrag(g + 1, bx[(g + 1) & 1], aw[(g + 1) & 1]);
    if ((g & 3) == 3 && s + 4 < 8) issue(s + 4);
    __builtin_amdgcn_sched_barrier(0);
#pragma unroll
    for (int a = 0; a < 2; ++a)
#pragma unroll
      for (int u = 0; u < 3; ++u)
        acc[a][u] = __builtin_amdgcn_mfma_f32_32x32x16_bf16(__builtin_bit_cast(bf16x8, aw[g & 1][u]),
                                                            __builtin_bit_cast(bf16x8, bx[g & 1][a]), acc[a][u], 0, 0, 0);
  }

#pragma unroll
  for (int a = 0; a < 2; ++a) {
#pragma unroll
    for (int u = 0; u < 3; ++u) {
      const int ug = ft * 6 + wf * 3 + u;
      if (ug < F_ / 16) {
        unsigned o[4];
#pragma unroll
        for (int p = 0; p < 4; ++p) {
          const unsigned gp = pack_bf16(acc[a][u][2 * p], acc[a][u][2 * p + 1]);
          const unsigned up = pack_bf16(acc[a][u][8 + 2 * p], acc[a][u][8 + 2 * p + 1]);
          const unsigned sp = pack_bf16(silu_f(bf_lo(gp)), silu_f(bf_hi(gp)));
          o[p] = pack_bf16(bf_lo(sp) * bf_lo(up), bf_hi(sp) * bf_hi(up));
        }
        const int m = m0 + wm * 64 + a * 32 + (lane & 31);
        const int off = (((m >> 4) * NS2 + (ug >> 1)) * 64 + (m & 15) + 16 * (2 * (ug & 1) + hh)) * 16;
        u32x4 v = {o[0], o[1], o[2], o[3]};
        *(u32x4*)(hws + off) = v;
      }
    }
  }
}

__global__ __launch_bounds__(256, 1) void k2_down(const unsigned char* __restrict__ hws,
                                                   const unsigned short* __restrict__ wd,
                                                   unsigned short* __restrict__ out) {
  __shared__ __attribute__((aligned(16))) f32x4 red[4 * 4 * 4 * 64];
  const int tid = threadIdx.x, lane = tid & 63;
  const int w = __builtin_amdgcn_readfirstlane(tid >> 6);
  const int b = blockIdx.x;
  const int xcd = b & 7, idx = b >> 3;
  const int mb = (xcd >> 1) * 8 + (idx & 7);
  const int nb = (xcd & 1) * 4 + (idx >> 3);
  const int m0 = mb * 64, n0 = nb * 64;
  const int s0 = w * 11;
  const int ns = (w == 3) ? 10 : 11;
  const int q = lane >> 4, i = lane & 15;

  __amdgpu_buffer_rsrc_t rh = __builtin_amdgcn_make_buffer_rsrc((void*)hws, 0, M_ * F_ * 2, 0x00020000);
  __amdgpu_buffer_rsrc_t rd = __builtin_amdgcn_make_buffer_rsrc((void*)wd, 0, F_ * D_ * 2, 0x00020000);

  const int dvoff = (16 * (q >> 1) + 4 * (q & 1)) * (D_ * 2) + (n0 + 4 * i) * 2;
  const int hvoff = lane * 16;
  u32x2 wr[11][8];
  u32x4 hb[11][4];
#pragma unroll
  for (int s = 0; s < 11; ++s) {
    const int sg = s0 + s;
#pragma unroll
    for (int j = 0; j < 8; ++j)
      wr[s][j] = __builtin_bit_cast(u32x2, __builtin_amdgcn_raw_buffer_load_b64(rd, dvoff, (sg * 32 + 8 * (j >> 2) + (j & 3)) * (D_ * 2), 0));
#pragma unroll
    for (int t = 0; t < 4; ++t) {
      const int sgc = sg < NS2 ? sg : NS2 - 1;
      hb[s][t] = __builtin_bit_cast(u32x4, __builtin_amdgcn_raw_buffer_load_b128(rh, hvoff, (((m0 >> 4) + ((t + w) & 3)) * NS2 + sgc) * 1024, 0));
    }
  }
  __builtin_amdgcn_sched_barrier(0);
  f32x4 acc[4][4];
#pragma unroll
  for (int t = 0; t < 4; ++t)
#pragma unroll
    for (int a = 0; a < 4; ++a) acc[t][a] = f32x4{0.f, 0.f, 0.f, 0.f};
#pragma unroll
  for (int s = 0; s < 11; ++s) {
    u32x4 af[4];
#pragma unroll
    for (int p = 0; p < 4; ++p) {
      const u32x2 A = wr[s][2 * p], B = wr[s][2 * p + 1];
      af[0][p] = __builtin_amdgcn_perm(B[0], A[0], 0x05040100u);
      af[1][p] = __builtin_amdgcn_perm(B[0], A[0], 0x07060302u);
      af[2][p] = __builtin_amdgcn_perm(B[1], A[1], 0x05040100u);
      af[3][p] = __builtin_amdgcn_perm(B[1], A[1], 0x07060302u);
    }
#pragma unroll
    for (int t = 0; t < 4; ++t)
#pragma unroll
      for (int a = 0; a < 4; ++a)
        acc[t][a] = __builtin_amdgcn_mfma_f32_16x16x32_bf16(__builtin_bit_cast(bf16x8, af[t]),
                                                            __builtin_bit_cast(bf16x8, hb[s][a]), acc[t][a], 0, 0, 0);
  }
  (void)ns;
#pragma unroll
  for (int a = 1; a < 4; ++a) {
#pragma unroll
    for (int t = 0; t < 4; ++t) red[((((w + a) & 3) * 4 + a) * 4 + t) * 64 + lane] = acc[t][a];
  }
  __syncthreads();
  f32x4 fin[4];
#pragma unroll
  for (int t = 0; t < 4; ++t) fin[t] = acc[t][0];
#pragma unroll
  for (int a = 1; a < 4; ++a) {
#pragma unroll
    for (int t = 0; t < 4; ++t) fin[t] += red[((w * 4 + a) * 4 + t) * 64 + lane];
  }
  unsigned o[8];
#pragma unroll
  for (int d = 0; d < 8; ++d) {
    const int r = d >> 1, t = 2 * (d & 1);
    o[d] = pack_bf16(fin[t][r], fin[t + 1][r]);
  }
  unsigned short* op = out + (size_t)(m0 + 16 * w + i) * D_ + n0 + 16 * q;
  *(u32x4*)op = u32x4{o[0], o[1], o[2], o[3]};
  *(u32x4*)(op + 8) = u32x4{o[4], o[5], o[6], o[7]};
}

extern "C" void kernel_launch(void* const* d_in, const int* in_sizes, int n_in, void* d_out, int out_size, void* d_ws,
                              size_t ws_size, hipStream_t stream) {
  const unsigned short* x = (const unsigned short*)d_in[0];
  const unsigned short* wg = (const unsigned short*)d_in[1];
  const unsigned short* wu = (const unsigned short*)d_in[2];
  const unsigned short* wd = (const unsigned short*)d_in[3];
  unsigned char* hws = (unsigned char*)d_ws;
  (void)hipFuncSetAttribute((const void*)k1_gate_up, hipFuncAttributeMaxDynamicSharedMemorySize, NSLOT * STG);
  k1_gate_up<<<240, 256, NSLOT * STG, stream>>>(x, wg, wu, hws);
  k2_down<<<256, 256, 0, stream>>>(hws, wd, (unsigned short*)d_out);
}
```

```cpp
#include <hip/hip_runtime.h>
#include <hip/hip_bf16.h>

typedef short s16x8 __attribute__((ext_vector_type(8)));
typedef short s16x4 __attribute__((ext_vector_type(4)));
typedef __bf16 bf16x8 __attribute__((ext_vector_type(8)));
typedef float f32x2 __attribute__((ext_vector_type(2)));
typedef float f32x4 __attribute__((ext_vector_type(4)));
typedef float f32x16 __attribute__((ext_vector_type(16)));
typedef unsigned u32x4 __attribute__((ext_vector_type(4)));
typedef unsigned u32x2 __attribute__((ext_vector_type(2)));
#define LAS __attribute__((address_space(3)))
#define GAS __attribute__((address_space(1)))

constexpr int M_ = 2048, D_ = 512, F_ = 1376;
constexpr int NS2 = F_ / 32;
constexpr int SLOT = 40960, STG_X = 16384, STG_W = 12288;
constexpr int LDS_BYTES = 163840;
constexpr unsigned FLAG_BYTES = 8192;
constexpr unsigned H_OFF = 65536;
constexpr unsigned H_BYTES = M_ * F_ * 2;
constexpr unsigned OOB = 0x40000000u;
constexpr int AUX_SC1 = 16;

#define STAMPS 0
#define STAMP(i)

__device__ __forceinline__ unsigned pack_bf16(float lo, float hi) {
  typedef __bf16 bf16x2 __attribute__((ext_vector_type(2)));
  f32x2 v = {lo, hi};
  bf16x2 b = __builtin_convertvector(v, bf16x2);
  return __builtin_bit_cast(unsigned, b);
}

#define GLDS(M0V, VOFF, RS, SOFF)                                                                    \
  asm volatile("s_mov_b32 m0, %0\n\ts_nop 0\n\tbuffer_load_dwordx4 %1, %2, %3 offen lds" ::"s"(M0V), \
               "v"(VOFF), "s"(RS), "s"(SOFF)                                                         \
               : "memory")
#define WAITV(n) asm volatile("s_waitcnt vmcnt(" #n ")" ::: "memory")
#define WAITL0() asm volatile("s_waitcnt lgkmcnt(0)" ::: "memory")
#define BAR()                              \
  do {                                     \
    asm volatile("" ::: "memory");         \
    __builtin_amdgcn_s_barrier();          \
    asm volatile("" ::: "memory");         \
  } while (0)
#define SCHED() __builtin_amdgcn_sched_barrier(0)

__device__ __forceinline__ u32x4 swiglu8(const f32x16& c) {
  u32x4 o;
#pragma unroll
  for (int p = 0; p < 4; ++p) {
    f32x2 g = {c[2 * p], c[2 * p + 1]};
    f32x2 u = {c[8 + 2 * p], c[8 + 2 * p + 1]};
    f32x2 t = g * -1.4426950408889634f;
    f32x2 e = {__builtin_amdgcn_exp2f(t[0]), __builtin_amdgcn_exp2f(t[1])};
    e = e + 1.0f;
    f32x2 r = {__builtin_amdgcn_rcpf(e[0]), __builtin_amdgcn_rcpf(e[1])};
    f32x2 h = (g * u) * r;
    o[p] = pack_bf16(h[0], h[1]);
  }
  return o;
}

__global__ __launch_bounds__(512, 2) void ffn_fused(const unsigned short* __restrict__ x,
                                                     const unsigned short* __restrict__ wg,
                                                     const unsigned short* __restrict__ wu,
                                                     const unsigned short* __restrict__ wd,
                                                     unsigned char* __restrict__ ws,
                                                     unsigned short* __restrict__ out) {
  extern __shared__ __attribute__((aligned(16))) unsigned char smem[];
  STAMP(0);
  const int tid = threadIdx.x, lane = tid & 63;
  const int w = __builtin_amdgcn_readfirstlane(tid >> 6);
  const int b = blockIdx.x;
  const int xcd = b & 7, idx = b >> 3;
  const int pl = idx & 3, jl = idx >> 2;
  const int p = 4 * (xcd >> 1) + pl;
  const int j = 8 * (xcd & 1) + jl;
  const int u0 = j < 6 ? 6 * j : 36 + 5 * (j - 6);
  const int nu = j < 6 ? 6 : 5;
  const int m0 = p * 128;
  const int rho = (jl + 2 * pl) & 7;
  const unsigned ldsb = (unsigned)(size_t)(LAS unsigned char*)smem;
  const LAS unsigned char* L = (const LAS unsigned char*)smem;

  __amdgpu_buffer_rsrc_t rh = __builtin_amdgcn_make_buffer_rsrc((void*)(ws + H_OFF), 0, H_BYTES, 0x00020000);
  GAS unsigned* flags = (GAS unsigned*)ws;

  const int kq = w & 3;
  const int mh = w >> 2;
  const int rb = 2 * p + (j >> 3);
  const int n0 = (j & 7) * 64;
  const int q = lane >> 4;

  if (w >= 4) {
    const int lw = w - 4;
    __amdgpu_buffer_rsrc_t rx = __builtin_amdgcn_make_buffer_rsrc((void*)x, 0, M_ * D_ * 2, 0x00020000);
    __amdgpu_buffer_rsrc_t rg = __builtin_amdgcn_make_buffer_rsrc((void*)wg, 0, D_ * F_ * 2, 0x00020000);
    __amdgpu_buffer_rsrc_t ru = __builtin_amdgcn_make_buffer_rsrc((void*)wu, 0, D_ * F_ * 2, 0x00020000);
    __amdgpu_buffer_rsrc_t rd = __builtin_amdgcn_make_buffer_rsrc((void*)wd, 0, F_ * D_ * 2, 0x00020000);
    int xoff[4];
#pragma unroll
    for (int i = 0; i < 4; ++i) {
      const int row = (lw * 4 + i) * 8 + (lane >> 3);
      const int ch = (lane & 7) ^ ((row >> 1) & 7);
      xoff[i] = (m0 + row) * (D_ * 2) + ch * 16;
    }
    int goff[3], uoff[3];
#pragma unroll
    for (int i = 0; i < 3; ++i) {
      const int c = (lw * 3 + i) * 64 + lane;
      const int k = c / 12, cc = c % 12;
      const int seg = cc >> 1, half = cc & 1;
      const int sg_ = seg, su_ = seg ^ 1;
      goff[i] = sg_ < nu ? k * (F_ * 2) + (u0 + sg_) * 32 + half * 16 : (int)OOB;
      uoff[i] = su_ < nu ? k * (F_ * 2) + (u0 + su_) * 32 + half * 16 : (int)OOB;
    }
    const unsigned m0x = ldsb + lw * 4096, m0g = ldsb + STG_X + lw * 3072, m0u = m0g + STG_W;
    auto issue = [&](int pos) {
      const unsigned sb = (pos % 3) * SLOT;
      const int st = (pos + rho) & 7;
      const unsigned sx = st * 128, sw = st * (64 * F_ * 2);
#pragma unroll
      for (int i = 0; i < 4; ++i) GLDS(m0x + sb + i * 1024, xoff[i], rx, sx);
#pragma unroll
      for (int i = 0; i < 3; ++i) GLDS(m0g + sb + i * 1024, goff[i], rg, sw);
#pragma unroll
      for (int i = 0; i < 3; ++i) GLDS(m0u + sb + i * 1024, uoff[i], ru, sw);
    };
    const int drow = lane >> 3;
    const int doff = (8 * lw + drow) * (D_ * 2) + n0 * 2 + ((((lane & 7) >> 1) ^ ((drow >> 1) & 3)) * 32) + (lane & 1) * 16;
    auto issue_wd = [&](int ridx, int sg) {
      const unsigned off = (ridx < 10 ? ridx + 30 : ridx - 10) * 4096;
      GLDS(ldsb + off + lw * 1024, doff, rd, (unsigned)sg * (32 * D_ * 2));
    };

    issue(0);
    issue(1);
    WAITV(10);
    BAR();
#pragma unroll
    for (int i = 0; i < 6; ++i) {
      issue(i + 2);
      WAITV(10);
      BAR();
    }
#pragma unroll
    for (int r = 0; r < 10; ++r) issue_wd(r, 11 * (r & 3) + (r >> 2));
    WAITV(10);
    BAR();
    BAR();
#pragma unroll 1
    for (int i = 0; i < 30; ++i) {
      int r = i + (pl * 30) / 4;
      r = (r >= 30 ? r - 30 : r) + 10;
      issue_wd(r, 11 * (r & 3) + (r >> 2));
    }
    WAITV(30);
    BAR();
  } else {
    const int wm = w >> 1, wf = w & 1;
    const int hh = lane >> 5;
    const int xsw = ((lane & 31) >> 1) & 7;
    const int xrow = (wm * 64 + (lane & 31)) * 128;
    int xk[4];
#pragma unroll
    for (int ks = 0; ks < 4; ++ks) xk[ks] = xrow + (((2 * ks + hh) ^ xsw) * 16);
    const int bsel = (lane >> 4) & 1;
    const int wbase = STG_X + bsel * STG_W + (8 * hh + ((lane >> 2) & 3)) * 192 + (lane & 3) * 8;
    int wa[3];
#pragma unroll
    for (int u = 0; u < 3; ++u) wa[u] = wbase + ((wf * 3 + u) ^ bsel) * 32;

    f32x16 acc[2][3];
#pragma unroll
    for (int a = 0; a < 2; ++a)
#pragma unroll
      for (int u = 0; u < 3; ++u)
#pragma unroll
        for (int r = 0; r < 16; ++r) acc[a][u][r] = 0.f;

    auto rdA = [&](int sb, int ks, int u) -> s16x8 {
      const LAS unsigned char* pp = L + sb + wa[u] + ks * (16 * 192);
      s16x4 lo = __builtin_amdgcn_ds_read_tr16_b64_v4i16((LAS s16x4*)pp);
      s16x4 hi = __builtin_amdgcn_ds_read_tr16_b64_v4i16((LAS s16x4*)(pp + 4 * 192));
      return __builtin_shufflevector(lo, hi, 0, 1, 2, 3, 4, 5, 6, 7);
    };
    auto rdB = [&](int sb, int ks, int a) -> s16x8 { return *(const LAS s16x8*)(L + sb + xk[ks] + a * 4096); };
#define MFMA32(A, B, C) __builtin_amdgcn_mfma_f32_32x32x16_bf16(__builtin_bit_cast(bf16x8, A), __builtin_bit_cast(bf16x8, B), C, 0, 0, 0)

    s16x8 fa[2][3], fb[2][2];
    BAR();
    STAMP(1);
#pragma unroll
    for (int u = 0; u < 3; ++u) fa[0][u] = rdA(0, 0, u);
#pragma unroll
    for (int a = 0; a < 2; ++a) fb[0][a] = rdB(0, 0, a);
#pragma unroll
    for (int g = 0; g < 28; ++g) {
      const int pos = g >> 2, ks = g & 3;
      if (ks == 3) {
        SCHED();
        WAITL0();
        BAR();
        STAMP(2 + pos);
      }
      SCHED();
      const int cur = g & 1, nxt = cur ^ 1;
      if (g + 1 < 28) {
        const int npos = (g + 1) >> 2, nks = (g + 1) & 3, sb = (npos % 3) * SLOT;
        fb[nxt][0] = rdB(sb, nks, 0);
#pragma unroll
        for (int u = 0; u < 3; ++u) fa[nxt][u] = rdA(sb, nks, u);
        fb[nxt][1] = rdB(sb, nks, 1);
#pragma unroll
        for (int a = 0; a < 2; ++a)
#pragma unroll
          for (int u = 0; u < 3; ++u) acc[a][u] = MFMA32(fa[cur][u], fb[cur][a], acc[a][u]);
#pragma unroll
        for (int i = 0; i < 4; ++i) {
          __builtin_amdgcn_sched_group_barrier(0x8, 1, 0);
          __builtin_amdgcn_sched_group_barrier(0x100, 2, 0);
        }
        __builtin_amdgcn_sched_group_barrier(0x8, 2, 0);
      }
    }
    s16x8 la[3][4], lb[2][4];
    {
      constexpr int sb = (7 % 3) * SLOT;
#pragma unroll
      for (int ks = 0; ks < 4; ++ks) {
        la[0][ks] = rdA(sb, ks, 0);
        lb[0][ks] = rdB(sb, ks, 0);
      }
#pragma unroll
      for (int a = 0; a < 2; ++a)
#pragma unroll
        for (int u = 0; u < 3; ++u) acc[a][u] = MFMA32(fa[1][u], fb[1][a], acc[a][u]);
#pragma unroll
      for (int i = 0; i < 6; ++i) {
        __builtin_amdgcn_sched_group_barrier(0x8, 1, 0);
        __builtin_amdgcn_sched_group_barrier(0x100, 2, 0);
      }
      SCHED();
#pragma unroll
      for (int ks = 0; ks < 4; ++ks) la[1][ks] = rdA(sb, ks, 1);
#pragma unroll
      for (int ks = 0; ks < 4; ++ks) acc[0][0] = MFMA32(la[0][ks], lb[0][ks], acc[0][0]);
#pragma unroll
      for (int i = 0; i < 4; ++i) {
        __builtin_amdgcn_sched_group_barrier(0x100, 2, 0);
        __builtin_amdgcn_sched_group_barrier(0x8, 1, 0);
      }
      SCHED();
#pragma unroll
      for (int ks = 0; ks < 4; ++ks) la[2][ks] = rdA(sb, ks, 2);
#pragma unroll
      for (int ks = 0; ks < 4; ++ks) lb[1][ks] = rdB(sb, ks, 1);
    }
    const int ugb = u0 + wf * 3;
    const int nuw = wf ? nu - 3 : 3;
    auto hstore = [&](int a, int u, u32x4 v) {
      const int m = m0 + wm * 64 + a * 32 + (lane & 31);
      const int ug = ugb + u;
      const int voff = ((m >> 4) * NS2 * 64 + (m & 15) + 16 * hh) * 16;
      const unsigned soff = u < nuw ? (unsigned)((ug >> 1) * 1024 + (ug & 1) * 512) : OOB;
      __builtin_amdgcn_raw_buffer_store_b128(v, rh, voff + (int)soff, 0, AUX_SC1);
    };
#pragma unroll
    for (int ks = 0; ks < 4; ++ks) acc[0][1] = MFMA32(la[1][ks], lb[0][ks], acc[0][1]);
    hstore(0, 0, swiglu8(acc[0][0]));
    SCHED();
    WAITL0();
    BAR();
    STAMP(9);
    SCHED();
#pragma unroll
    for (int ks = 0; ks < 4; ++ks) acc[0][2] = MFMA32(la[2][ks], lb[0][ks], acc[0][2]);
    hstore(0, 1, swiglu8(acc[0][1]));
    SCHED();
#pragma unroll
    for (int ks = 0; ks < 4; ++ks) acc[1][0] = MFMA32(la[0][ks], lb[1][ks], acc[1][0]);
    hstore(0, 2, swiglu8(acc[0][2]));
    SCHED();
#pragma unroll
    for (int ks = 0; ks < 4; ++ks) acc[1][1] = MFMA32(la[1][ks], lb[1][ks], acc[1][1]);
    hstore(1, 0, swiglu8(acc[1][0]));
    SCHED();
#pragma unroll
    for (int ks = 0; ks < 4; ++ks) acc[1][2] = MFMA32(la[2][ks], lb[1][ks], acc[1][2]);
    hstore(1, 1, swiglu8(acc[1][1]));
    SCHED();
    GAS unsigned* fl = flags + ((p * 2 + wm) * 2) * 32 + j * 2 + wf;
    WAITV(2);
    if (lane == 0) __hip_atomic_store(fl, 1u, __ATOMIC_RELAXED, __HIP_MEMORY_SCOPE_AGENT);
    SCHED();
    hstore(1, 2, swiglu8(acc[1][2]));
    STAMP(10);
    WAITV(0);
    if (lane == 0) __hip_atomic_store(fl + 32, 1u, __ATOMIC_RELAXED, __HIP_MEMORY_SCOPE_AGENT);
    STAMP(11);
    BAR();
  }

  int aoff[4];
  {
    const int qp = (lane >> 2) & 3, pp = lane & 3;
    const int row = 16 * (q >> 1) + 4 * (q & 1) + qp;
    const int g = 2 * (q & 1) + (qp >> 1);
#pragma unroll
    for (int t = 0; t < 4; ++t) {
      const int nt = t ^ (2 * (kq & 1));
      aoff[t] = row * 128 + ((nt ^ g) * 32) + pp * 8;
    }
  }
  auto rdW = [&](int roff, int t) -> s16x8 {
    const LAS unsigned char* pp = L + roff + aoff[t];
    s16x4 lo = __builtin_amdgcn_ds_read_tr16_b64_v4i16((LAS s16x4*)pp);
    s16x4 hi = __builtin_amdgcn_ds_read_tr16_b64_v4i16((LAS s16x4*)(pp + 1024));
    return __builtin_shufflevector(lo, hi, 0, 1, 2, 3, 4, 5, 6, 7);
  };
  const int roff0 = (30 + kq) * 4096;
  s16x8 a0[4];
#pragma unroll
  for (int t = 0; t < 4; ++t) a0[t] = rdW(roff0, t);
  WAITL0();
  BAR();
  STAMP(12);

  const int pi = (lane >> 1) & 15, pwf = lane & 1;
  const int pu0 = (pi < 6 ? 6 * pi : 36 + 5 * (pi - 6)) + 3 * pwf;
  const int pue = pwf ? (pi < 6 ? 6 * pi + 6 : 36 + 5 * (pi - 6) + 5) : pu0 + 3;
  const bool need = lane < 32 && pu0 < 22 * kq + 22 && pue > 22 * kq;
  GAS unsigned* pf = flags + ((p * 2 + (j >> 3)) * 2 + mh) * 32 + (lane & 31);
  auto poll = [&]() {
#pragma unroll 1
    for (int spin = 0; spin < (1 << 16); ++spin) {
      const unsigned v = __hip_atomic_load(pf, __ATOMIC_RELAXED, __HIP_MEMORY_SCOPE_AGENT);
      if (__all(!need || v == 1u)) break;
      __builtin_amdgcn_s_sleep(1);
    }
    __builtin_amdgcn_fence(__ATOMIC_ACQUIRE, "wavefront");
  };
  const int rot = ((j & 7) * 9) >> 3;
  u32x4 hb[11][2];
  auto hloads = [&]() {
#pragma unroll
    for (int i = 0; i < 11; ++i) {
      int rel;
      if (i == 0) rel = 0;
      else if (i == 10) rel = 10;
      else { rel = i - 1 + rot; rel = (rel >= 9 ? rel - 9 : rel) + 1; }
      const int sg = 11 * kq + rel;
#pragma unroll
      for (int a = 0; a < 2; ++a) {
        const int mt = 4 * rb + 2 * mh + (a ^ (kq >> 1));
        unsigned so = (unsigned)(mt * NS2 + sg) * 1024;
        if (i == 10) so |= (unsigned)(kq == 3) << 30;
        hb[i][a] = __builtin_bit_cast(u32x4, __builtin_amdgcn_raw_buffer_load_b128(rh, lane * 16, so, AUX_SC1));
      }
    }
    SCHED();
  };

  if (w >= 4) {
    const int lw = w - 4;
    __amdgpu_buffer_rsrc_t rd = __builtin_amdgcn_make_buffer_rsrc((void*)wd, 0, F_ * D_ * 2, 0x00020000);
    const int drow = lane >> 3;
    const int doff = (8 * lw + drow) * (D_ * 2) + n0 * 2 + ((((lane & 7) >> 1) ^ ((drow >> 1) & 3)) * 32) + (lane & 1) * 16;
#pragma unroll
    for (int r = 0; r < 3; ++r) GLDS(ldsb + (30 + r) * 4096 + lw * 1024, doff, rd, (unsigned)(11 * r + 10) * (32 * D_ * 2));
    WAITV(0);
    BAR();
  }
  poll();
  STAMP(13);
  hloads();
  if (w < 4) BAR();
  STAMP(14);

  f32x4 acc2[4][2];
#pragma unroll
  for (int t = 0; t < 4; ++t)
#pragma unroll
    for (int a = 0; a < 2; ++a) acc2[t][a] = f32x4{0.f, 0.f, 0.f, 0.f};
#define MFMA16(A, B, C) __builtin_amdgcn_mfma_f32_16x16x32_bf16(__builtin_bit_cast(bf16x8, A), __builtin_bit_cast(bf16x8, B), C, 0, 0, 0)
  s16x8 af[2][4];
#pragma unroll
  for (int t = 0; t < 4; ++t) af[0][t] = a0[t];
#pragma unroll
  for (int i = 0; i < 11; ++i) {
    const int cur = i & 1, nxt = cur ^ 1;
    if (i + 1 < 11) {
      int roff;
      if (i + 1 == 10) roff = roff0;
      else {
        int rel = i + rot;
        rel = (rel >= 9 ? rel - 9 : rel) + 1;
        const int ridx = rel * 4 + kq;
        roff = (ridx < 10 ? ridx + 30 : ridx - 10) * 4096;
      }
#pragma unroll
      for (int t = 0; t < 4; ++t) af[nxt][t] = rdW(roff, t);
    }
#pragma unroll
    for (int a = 0; a < 2; ++a)
#pragma unroll
      for (int t = 0; t < 4; ++t) acc2[t][a] = MFMA16(af[cur][t], hb[i][a], acc2[t][a]);
    SCHED();
  }
  STAMP(15);
  BAR();
  LAS f32x4* red = (LAS f32x4*)smem;
#pragma unroll
  for (int g = 1; g < 4; ++g) {
    const int a = g >> 1, tp = g & 1;
#pragma unroll
    for (int t0 = 0; t0 < 2; ++t0) red[((((mh * 4 + (kq ^ g)) * 3 + (g - 1)) * 2 + t0) * 64) + lane] = acc2[2 * tp + t0][a];
  }
  WAITL0();
  BAR();
  f32x4 f0 = acc2[0][0], f1 = acc2[1][0];
#pragma unroll
  for (int g = 1; g < 4; ++g) {
    f0 += red[((((mh * 4 + kq) * 3 + (g - 1)) * 2 + 0) * 64) + lane];
    f1 += red[((((mh * 4 + kq) * 3 + (g - 1)) * 2 + 1) * 64) + lane];
  }
  {
    unsigned short* op = out + (size_t)(rb * 64 + 16 * (2 * mh + (kq >> 1)) + (lane & 15)) * D_ + n0 + 32 * (kq & 1) + 4 * q;
    *(u32x2*)op = u32x2{pack_bf16(f0[0], f0[1]), pack_bf16(f0[2], f0[3])};
    *(u32x2*)(op + 16) = u32x2{pack_bf16(f1[0], f1[1]), pack_bf16(f1[2], f1[3])};
  }
  STAMP(16);
}

extern "C" void kernel_launch(void* const* d_in, const int* in_sizes, int n_in, void* d_out, int out_size, void* d_ws,
                              size_t ws_size, hipStream_t stream) {
  const unsigned short* x = (const unsigned short*)d_in[0];
  const unsigned short* wg = (const unsigned short*)d_in[1];
  const unsigned short* wu = (const unsigned short*)d_in[2];
  const unsigned short* wd = (const unsigned short*)d_in[3];
  (void)hipFuncSetAttribute((const void*)ffn_fused, hipFuncAttributeMaxDynamicSharedMemorySize, LDS_BYTES);
  (void)hipMemsetAsync(d_ws, 0, FLAG_BYTES, stream);
  ffn_fused<<<256, 512, LDS_BYTES, stream>>>(x, wg, wu, wd, (unsigned char*)d_ws, (unsigned short*)d_out);
}
```

```cpp
#include <hip/hip_runtime.h>
#include <hip/hip_bf16.h>

typedef short s16x8 __attribute__((ext_vector_type(8)));
typedef short s16x4 __attribute__((ext_vector_type(4)));
typedef __bf16 bf16x8 __attribute__((ext_vector_type(8)));
typedef float f32x2 __attribute__((ext_vector_type(2)));
typedef float f32x4 __attribute__((ext_vector_type(4)));
typedef float f32x16 __attribute__((ext_vector_type(16)));
typedef unsigned u32x4 __attribute__((ext_vector_type(4)));
typedef unsigned u32x2 __attribute__((ext_vector_type(2)));
#define LAS __attribute__((address_space(3)))
#define GAS __attribute__((address_space(1)))

constexpr int M_ = 2048, D_ = 512, F_ = 1376;
constexpr int NS2 = F_ / 32;
constexpr int SLOT = 40960, STG_X = 16384, STG_W = 12288;
constexpr int LDS_BYTES = 163840;
constexpr unsigned CB_PANEL = 8192, CB_N = 0, CB_FLAG = 512, CB_E = 2048, CB_V = 4096;
constexpr unsigned H_OFF = 16 * CB_PANEL;
constexpr unsigned H_BYTES = M_ * F_ * 2;
constexpr unsigned OOB = 0x40000000u;
constexpr int AUX_SC1 = 16;

#define STAMPS 0
#define STAMP(i)

__device__ __forceinline__ unsigned pack_bf16(float lo, float hi) {
  typedef __bf16 bf16x2 __attribute__((ext_vector_type(2)));
  f32x2 v = {lo, hi};
  bf16x2 b = __builtin_convertvector(v, bf16x2);
  return __builtin_bit_cast(unsigned, b);
}

#define GLDS(M0V, VOFF, RS, SOFF)                                                                    \
  asm volatile("s_mov_b32 m0, %0\n\ts_nop 0\n\tbuffer_load_dwordx4 %1, %2, %3 offen lds" ::"s"(M0V), \
               "v"(VOFF), "s"(RS), "s"(SOFF)                                                         \
               : "memory")
#define WAITV(n) asm volatile("s_waitcnt vmcnt(" #n ")" ::: "memory")
#define WAITL0() asm volatile("s_waitcnt lgkmcnt(0)" ::: "memory")
#define BAR()                              \
  do {                                     \
    asm volatile("" ::: "memory");         \
    __builtin_amdgcn_s_barrier();          \
    asm volatile("" ::: "memory");         \
  } while (0)
#define SCHED() __builtin_amdgcn_sched_barrier(0)

__device__ __forceinline__ u32x4 swiglu8(const f32x16& c) {
  u32x4 o;
#pragma unroll
  for (int p = 0; p < 4; ++p) {
    f32x2 g = {c[2 * p], c[2 * p + 1]};
    f32x2 u = {c[8 + 2 * p], c[8 + 2 * p + 1]};
    f32x2 t = g * -1.4426950408889634f;
    f32x2 e = {__builtin_amdgcn_exp2f(t[0]), __builtin_amdgcn_exp2f(t[1])};
    e = e + 1.0f;
    f32x2 r = {__builtin_amdgcn_rcpf(e[0]), __builtin_amdgcn_rcpf(e[1])};
    f32x2 h = (g * u) * r;
    o[p] = pack_bf16(h[0], h[1]);
  }
  return o;
}

__global__ __launch_bounds__(512, 2) void ffn_fused(const unsigned short* __restrict__ x,
                                                     const unsigned short* __restrict__ wg,
                                                     const unsigned short* __restrict__ wu,
                                                     const unsigned short* __restrict__ wd,
                                                     unsigned char* __restrict__ ws,
                                                     unsigned short* __restrict__ out) {
  extern __shared__ __attribute__((aligned(16))) unsigned char smem[];
  STAMP(0);
  const int tid = threadIdx.x, lane = tid & 63;
  const int w = __builtin_amdgcn_readfirstlane(tid >> 6);
  const int b = blockIdx.x;
  const int xcd = b & 7, idx = b >> 3;
  const int pl = idx & 3, jl = idx >> 2;
  const int p = 4 * (xcd >> 1) + pl;
  const int j = 8 * (xcd & 1) + jl;
  const int u0 = j < 6 ? 6 * j : 36 + 5 * (j - 6);
  const int nu = j < 6 ? 6 : 5;
  const int m0 = p * 128;
  const int rho = (jl + 2 * pl) & 7;
  const unsigned ldsb = (unsigned)(size_t)(LAS unsigned char*)smem;
  const LAS unsigned char* L = (const LAS unsigned char*)smem;

  __amdgpu_buffer_rsrc_t rh = __builtin_amdgcn_make_buffer_rsrc((void*)(ws + H_OFF), 0, H_BYTES, 0x00020000);
  GAS unsigned char* cb = (GAS unsigned char*)ws + p * CB_PANEL;

  const int kq = w & 3;
  const int mh = w >> 2;
  const int rb = 2 * p + (j >> 3);
  const int n0 = (j & 7) * 64;
  const int q = lane >> 4;

  if (w >= 4) {
    const int lw = w - 4;
    __amdgpu_buffer_rsrc_t rx = __builtin_amdgcn_make_buffer_rsrc((void*)x, 0, M_ * D_ * 2, 0x00020000);
    __amdgpu_buffer_rsrc_t rg = __builtin_amdgcn_make_buffer_rsrc((void*)wg, 0, D_ * F_ * 2, 0x00020000);
    __amdgpu_buffer_rsrc_t ru = __builtin_amdgcn_make_buffer_rsrc((void*)wu, 0, D_ * F_ * 2, 0x00020000);
    __amdgpu_buffer_rsrc_t rd = __builtin_amdgcn_make_buffer_rsrc((void*)wd, 0, F_ * D_ * 2, 0x00020000);
    int xoff[4];
#pragma unroll
    for (int i = 0; i < 4; ++i) {
      const int row = (lw * 4 + i) * 8 + (lane >> 3);
      const int ch = (lane & 7) ^ ((row >> 1) & 7);
      xoff[i] = (m0 + row) * (D_ * 2) + ch * 16;
    }
    int goff[3], uoff[3];
#pragma unroll
    for (int i = 0; i < 3; ++i) {
      const int c = (lw * 3 + i) * 64 + lane;
      const int k = c / 12, cc = c % 12;
      const int seg = cc >> 1, half = cc & 1;
      const int sg_ = seg, su_ = seg ^ 1;
      goff[i] = sg_ < nu ? k * (F_ * 2) + (u0 + sg_) * 32 + half * 16 : (int)OOB;
      uoff[i] = su_ < nu ? k * (F_ * 2) + (u0 + su_) * 32 + half * 16 : (int)OOB;
    }
    const unsigned m0x = ldsb + lw * 4096, m0g = ldsb + STG_X + lw * 3072, m0u = m0g + STG_W;
    auto issue = [&](int pos) {
      const unsigned sb = (pos % 3) * SLOT;
      const int st = (pos + rho) & 7;
      const unsigned sx = st * 128, sw = st * (64 * F_ * 2);
#pragma unroll
      for (int i = 0; i < 4; ++i) GLDS(m0x + sb + i * 1024, xoff[i], rx, sx);
#pragma unroll
      for (int i = 0; i < 3; ++i) GLDS(m0g + sb + i * 1024, goff[i], rg, sw);
#pragma unroll
      for (int i = 0; i < 3; ++i) GLDS(m0u + sb + i * 1024, uoff[i], ru, sw);
    };
    const int drow = lane >> 3;
    const int doff = (8 * lw + drow) * (D_ * 2) + n0 * 2 + ((((lane & 7) >> 1) ^ ((drow >> 1) & 3)) * 32) + (lane & 1) * 16;
    auto issue_wd = [&](int ridx, int sg) {
      const unsigned off = (ridx < 10 ? ridx + 30 : ridx - 10) * 4096;
      GLDS(ldsb + off + lw * 1024, doff, rd, (unsigned)sg * (32 * D_ * 2));
    };

    const bool hs = (lw == 3);
    unsigned myn = 0, pn = 0, echoed = 0;
    bool ver = false, first = true;
    u32x2 hv = {0u, 0u};
    GAS unsigned char* gaddr = cb + CB_N + j * 8;
    if (lane < 16) gaddr = cb + CB_E + j * 128 + lane * 8;
    else if (lane < 32) gaddr = cb + CB_N + (lane - 16) * 8;
    else if (lane < 36) gaddr = cb + CB_FLAG + (lane - 32) * 128 + j * 8;
#define HS_LOAD() asm volatile("global_load_dwordx2 %0, %1, off sc1" : "+v"(hv) : "v"(gaddr) : "memory")
#define WAITV_HV(n) asm volatile("s_waitcnt vmcnt(" #n ")" : "+v"(hv)::"memory")
    auto hs_round = [&]() -> bool {
      const unsigned nrow = __shfl(hv[0], (lane & 15) + 16);
      if (!ver && hv[0] == myn) { ver = true; pn = hv[1]; }
      const unsigned target = ver ? pn : nrow;
      if (lane < 16 && (first || target != echoed)) {
        __hip_atomic_store((GAS unsigned long long*)(cb + CB_E + lane * 128 + j * 8),
                           ((unsigned long long)myn << 32) | target, __ATOMIC_RELAXED, __HIP_MEMORY_SCOPE_AGENT);
      }
      echoed = target;
      first = false;
      return __all(lane >= 16 || ver);
    };

    HS_LOAD();
    issue(0);
    issue(1);
    WAITV_HV(10);
    if (hs) {
      const unsigned base = __shfl(hv[0], 63);
      const bool mine = lane < 16 || (lane >= 32 && lane < 36);
      unsigned c = base;
#pragma unroll 1
      do { ++c; } while (__any(mine && (hv[0] == c || hv[1] == c)));
      myn = c;
      if (lane == 0)
        __hip_atomic_store((GAS unsigned long long*)(cb + CB_N + j * 8), (unsigned long long)myn, __ATOMIC_RELAXED, __HIP_MEMORY_SCOPE_AGENT);
    }
    BAR();
    bool hdone = false;
#pragma unroll
    for (int i = 0; i < 6; ++i) {
      if (i > 0 && hs && !hdone) hdone = hs_round();
      HS_LOAD();
      issue(i + 2);
      WAITV_HV(10);
      BAR();
    }
#pragma unroll
    for (int r = 0; r < 10; ++r) issue_wd(r, 11 * (r & 3) + (r >> 2));
    WAITV(10);
    BAR();
    BAR();
    if (hs) {
#pragma unroll 1
      for (int spin = 0; spin < (1 << 16) && !hdone; ++spin) {
        hdone = hs_round();
        if (!hdone) {
          __builtin_amdgcn_s_sleep(1);
          asm volatile("global_load_dwordx2 %0, %1, off sc1\n\ts_waitcnt vmcnt(0)" : "=&v"(hv) : "v"(gaddr) : "memory");
        }
      }
      if (lane < 16) __hip_atomic_store((GAS unsigned*)(cb + CB_V + j * 128 + lane * 4), pn, __ATOMIC_RELAXED, __HIP_MEMORY_SCOPE_AGENT);
    }
#pragma unroll 1
    for (int i = 0; i < 30; ++i) {
      int r = i + (pl * 30) / 4;
      r = (r >= 30 ? r - 30 : r) + 10;
      issue_wd(r, 11 * (r & 3) + (r >> 2));
    }
    WAITV(30);
    BAR();
  } else {
    const int wm = w >> 1, wf = w & 1;
    const int hh = lane >> 5;
    const int xsw = ((lane & 31) >> 1) & 7;
    const int xrow = (wm * 64 + (lane & 31)) * 128;
    int xk[4];
#pragma unroll
    for (int ks = 0; ks < 4; ++ks) xk[ks] = xrow + (((2 * ks + hh) ^ xsw) * 16);
    const int bsel = (lane >> 4) & 1;
    const int wbase = STG_X + bsel * STG_W + (8 * hh + ((lane >> 2) & 3)) * 192 + (lane & 3) * 8;
    int wa[3];
#pragma unroll
    for (int u = 0; u < 3; ++u) wa[u] = wbase + ((wf * 3 + u) ^ bsel) * 32;

    f32x16 acc[2][3];
#pragma unroll
    for (int a = 0; a < 2; ++a)
#pragma unroll
      for (int u = 0; u < 3; ++u)
#pragma unroll
        for (int r = 0; r < 16; ++r) acc[a][u][r] = 0.f;

    auto rdA = [&](int sb, int ks, int u) -> s16x8 {
      const LAS unsigned char* pp = L + sb + wa[u] + ks * (16 * 192);
      s16x4 lo = __builtin_amdgcn_ds_read_tr16_b64_v4i16((LAS s16x4*)pp);
      s16x4 hi = __builtin_amdgcn_ds_read_tr16_b64_v4i16((LAS s16x4*)(pp + 4 * 192));
      return __builtin_shufflevector(lo, hi, 0, 1, 2, 3, 4, 5, 6, 7);
    };
    auto rdB = [&](int sb, int ks, int a) -> s16x8 { return *(const LAS s16x8*)(L + sb + xk[ks] + a * 4096); };
#define MFMA32(A, B, C) __builtin_amdgcn_mfma_f32_32x32x16_bf16(__builtin_bit_cast(bf16x8, A), __builtin_bit_cast(bf16x8, B), C, 0, 0, 0)

    s16x8 fa[2][3], fb[2][2];
    unsigned ownn = 0;
    BAR();
    STAMP(1);
#pragma unroll
    for (int u = 0; u < 3; ++u) fa[0][u] = rdA(0, 0, u);
#pragma unroll
    for (int a = 0; a < 2; ++a) fb[0][a] = rdB(0, 0, a);
#pragma unroll
    for (int g = 0; g < 28; ++g) {
      const int pos = g >> 2, ks = g & 3;
      if (ks == 3) {
        SCHED();
        WAITL0();
        BAR();
        STAMP(2 + pos);
        if (pos == 2)
          ownn = __hip_atomic_load((GAS unsigned*)(cb + CB_N + j * 8), __ATOMIC_RELAXED, __HIP_MEMORY_SCOPE_AGENT);
      }
      SCHED();
      const int cur = g & 1, nxt = cur ^ 1;
      if (g + 1 < 28) {
        const int npos = (g + 1) >> 2, nks = (g + 1) & 3, sb = (npos % 3) * SLOT;
        fb[nxt][0] = rdB(sb, nks, 0);
#pragma unroll
        for (int u = 0; u < 3; ++u) fa[nxt][u] = rdA(sb, nks, u);
        fb[nxt][1] = rdB(sb, nks, 1);
#pragma unroll
        for (int a = 0; a < 2; ++a)
#pragma unroll
          for (int u = 0; u < 3; ++u) acc[a][u] = MFMA32(fa[cur][u], fb[cur][a], acc[a][u]);
#pragma unroll
        for (int i = 0; i < 4; ++i) {
          __builtin_amdgcn_sched_group_barrier(0x8, 1, 0);
          __builtin_amdgcn_sched_group_barrier(0x100, 2, 0);
        }
        __builtin_amdgcn_sched_group_barrier(0x8, 2, 0);
      }
    }
    s16x8 la[3][4], lb[2][4];
    {
      constexpr int sb = (7 % 3) * SLOT;
#pragma unroll
      for (int ks = 0; ks < 4; ++ks) {
        la[0][ks] = rdA(sb, ks, 0);
        lb[0][ks] = rdB(sb, ks, 0);
      }
#pragma unroll
      for (int a = 0; a < 2; ++a)
#pragma unroll
        for (int u = 0; u < 3; ++u) acc[a][u] = MFMA32(fa[1][u], fb[1][a], acc[a][u]);
#pragma unroll
      for (int i = 0; i < 6; ++i) {
        __builtin_amdgcn_sched_group_barrier(0x8, 1, 0);
        __builtin_amdgcn_sched_group_barrier(0x100, 2, 0);
      }
      SCHED();
#pragma unroll
      for (int ks = 0; ks < 4; ++ks) la[1][ks] = rdA(sb, ks, 1);
#pragma unroll
      for (int ks = 0; ks < 4; ++ks) acc[0][0] = MFMA32(la[0][ks], lb[0][ks], acc[0][0]);
#pragma unroll
      for (int i = 0; i < 4; ++i) {
        __builtin_amdgcn_sched_group_barrier(0x100, 2, 0);
        __builtin_amdgcn_sched_group_barrier(0x8, 1, 0);
      }
      SCHED();
#pragma unroll
      for (int ks = 0; ks < 4; ++ks) la[2][ks] = rdA(sb, ks, 2);
#pragma unroll
      for (int ks = 0; ks < 4; ++ks) lb[1][ks] = rdB(sb, ks, 1);
    }
    const int ugb = u0 + wf * 3;
    const int nuw = wf ? nu - 3 : 3;
    auto hstore = [&](int a, int u, u32x4 v) {
      const int m = m0 + wm * 64 + a * 32 + (lane & 31);
      const int ug = ugb + u;
      const int voff = ((m >> 4) * NS2 * 64 + (m & 15) + 16 * hh) * 16;
      const unsigned soff = u < nuw ? (unsigned)((ug >> 1) * 1024 + (ug & 1) * 512) : OOB;
      __builtin_amdgcn_raw_buffer_store_b128(v, rh, voff + (int)soff, 0, AUX_SC1);
    };
#pragma unroll
    for (int ks = 0; ks < 4; ++ks) acc[0][1] = MFMA32(la[1][ks], lb[0][ks], acc[0][1]);
    hstore(0, 0, swiglu8(acc[0][0]));
    SCHED();
    WAITL0();
    BAR();
    STAMP(9);
    SCHED();
#pragma unroll
    for (int ks = 0; ks < 4; ++ks) acc[0][2] = MFMA32(la[2][ks], lb[0][ks], acc[0][2]);
    hstore(0, 1, swiglu8(acc[0][1]));
    SCHED();
#pragma unroll
    for (int ks = 0; ks < 4; ++ks) acc[1][0] = MFMA32(la[0][ks], lb[1][ks], acc[1][0]);
    hstore(0, 2, swiglu8(acc[0][2]));
    SCHED();
#pragma unroll
    for (int ks = 0; ks < 4; ++ks) acc[1][1] = MFMA32(la[1][ks], lb[1][ks], acc[1][1]);
    hstore(1, 0, swiglu8(acc[1][0]));
    SCHED();
#pragma unroll
    for (int ks = 0; ks < 4; ++ks) acc[1][2] = MFMA32(la[2][ks], lb[1][ks], acc[1][2]);
    hstore(1, 1, swiglu8(acc[1][1]));
    SCHED();
    GAS unsigned* fl = (GAS unsigned*)(cb + CB_FLAG) + (wm * 2) * 32 + j * 2 + wf;
    WAITV(2);
    if (lane == 0) __hip_atomic_store(fl, ownn, __ATOMIC_RELAXED, __HIP_MEMORY_SCOPE_AGENT);
    SCHED();
    hstore(1, 2, swiglu8(acc[1][2]));
    STAMP(10);
    WAITV(0);
    if (lane == 0) __hip_atomic_store(fl + 32, ownn, __ATOMIC_RELAXED, __HIP_MEMORY_SCOPE_AGENT);
    STAMP(11);
    BAR();
  }

  int aoff[4];
  {
    const int qp = (lane >> 2) & 3, pp = lane & 3;
    const int row = 16 * (q >> 1) + 4 * (q & 1) + qp;
    const int g = 2 * (q & 1) + (qp >> 1);
#pragma unroll
    for (int t = 0; t < 4; ++t) {
      const int nt = t ^ (2 * (kq & 1));
      aoff[t] = row * 128 + ((nt ^ g) * 32) + pp * 8;
    }
  }
  auto rdW = [&](int roff, int t) -> s16x8 {
    const LAS unsigned char* pp = L + roff + aoff[t];
    s16x4 lo = __builtin_amdgcn_ds_read_tr16_b64_v4i16((LAS s16x4*)pp);
    s16x4 hi = __builtin_amdgcn_ds_read_tr16_b64_v4i16((LAS s16x4*)(pp + 1024));
    return __builtin_shufflevector(lo, hi, 0, 1, 2, 3, 4, 5, 6, 7);
  };
  const int roff0 = (30 + kq) * 4096;
  s16x8 a0[4];
#pragma unroll
  for (int t = 0; t < 4; ++t) a0[t] = rdW(roff0, t);
  WAITL0();
  BAR();
  STAMP(12);

  const int pi = (lane >> 1) & 15, pwf = lane & 1;
  const int pu0 = (pi < 6 ? 6 * pi : 36 + 5 * (pi - 6)) + 3 * pwf;
  const int pue = pwf ? (pi < 6 ? 6 * pi + 6 : 36 + 5 * (pi - 6) + 5) : pu0 + 3;
  const bool need = lane < 32 && pu0 < 22 * kq + 22 && pue > 22 * kq;
  GAS unsigned* pf = (GAS unsigned*)(cb + CB_FLAG) + ((j >> 3) * 2 + mh) * 32 + (lane & 31);
  const unsigned want = __hip_atomic_load((GAS unsigned*)(cb + CB_V + j * 128 + pi * 4), __ATOMIC_RELAXED, __HIP_MEMORY_SCOPE_AGENT);
  auto poll = [&]() {
#pragma unroll 1
    for (int spin = 0; spin < (1 << 16); ++spin) {
      const unsigned v = __hip_atomic_load(pf, __ATOMIC_RELAXED, __HIP_MEMORY_SCOPE_AGENT);
      if (__all(!need || v == want)) break;
      __builtin_amdgcn_s_sleep(1);
    }
    __builtin_amdgcn_fence(__ATOMIC_ACQUIRE, "wavefront");
  };
  const int rot = ((j & 7) * 9) >> 3;
  u32x4 hb[11][2];
  auto hloads = [&]() {
#pragma unroll
    for (int i = 0; i < 11; ++i) {
      int rel;
      if (i == 0) rel = 0;
      else if (i == 10) rel = 10;
      else { rel = i - 1 + rot; rel = (rel >= 9 ? rel - 9 : rel) + 1; }
      const int sg = 11 * kq + rel;
#pragma unroll
      for (int a = 0; a < 2; ++a) {
        const int mt = 4 * rb + 2 * mh + (a ^ (kq >> 1));
        unsigned so = (unsigned)(mt * NS2 + sg) * 1024;
        if (i == 10) so |= (unsigned)(kq == 3) << 30;
        hb[i][a] = __builtin_bit_cast(u32x4, __builtin_amdgcn_raw_buffer_load_b128(rh, lane * 16, so, AUX_SC1));
      }
    }
    SCHED();
  };

  if (w >= 4) {
    const int lw = w - 4;
    __amdgpu_buffer_rsrc_t rd = __builtin_amdgcn_make_buffer_rsrc((void*)wd, 0, F_ * D_ * 2, 0x00020000);
    const int drow = lane >> 3;
    const int doff = (8 * lw + drow) * (D_ * 2) + n0 * 2 + ((((lane & 7) >> 1) ^ ((drow >> 1) & 3)) * 32) + (lane & 1) * 16;
#pragma unroll
    for (int r = 0; r < 3; ++r) GLDS(ldsb + (30 + r) * 4096 + lw * 1024, doff, rd, (unsigned)(11 * r + 10) * (32 * D_ * 2));
    WAITV(0);
    BAR();
  }
  poll();
  STAMP(13);
  hloads();
  if (w < 4) BAR();
  STAMP(14);

  f32x4 acc2[4][2];
#pragma unroll
  for (int t = 0; t < 4; ++t)
#pragma unroll
    for (int a = 0; a < 2; ++a) acc2[t][a] = f32x4{0.f, 0.f, 0.f, 0.f};
#define MFMA16(A, B, C) __builtin_amdgcn_mfma_f32_16x16x32_bf16(__builtin_bit_cast(bf16x8, A), __builtin_bit_cast(bf16x8, B), C, 0, 0, 0)
  s16x8 af[2][4];
#pragma unroll
  for (int t = 0; t < 4; ++t) af[0][t] = a0[t];
#pragma unroll
  for (int i = 0; i < 11; ++i) {
    const int cur = i & 1, nxt = cur ^ 1;
    if (i + 1 < 11) {
      int roff;
      if (i + 1 == 10) roff = roff0;
      else {
        int rel = i + rot;
        rel = (rel >= 9 ? rel - 9 : rel) + 1;
        const int ridx = rel * 4 + kq;
        roff = (ridx < 10 ? ridx + 30 : ridx - 10) * 4096;
      }
#pragma unroll
      for (int t = 0; t < 4; ++t) af[nxt][t] = rdW(roff, t);
    }
#pragma unroll
    for (int a = 0; a < 2; ++a)
#pragma unroll
      for (int t = 0; t < 4; ++t) acc2[t][a] = MFMA16(af[cur][t], hb[i][a], acc2[t][a]);
    SCHED();
  }
  STAMP(15);
  BAR();
  LAS f32x4* red = (LAS f32x4*)smem;
#pragma unroll
  for (int g = 1; g < 4; ++g) {
    const int a = g >> 1, tp = g & 1;
#pragma unroll
    for (int t0 = 0; t0 < 2; ++t0) red[((((mh * 4 + (kq ^ g)) * 3 + (g - 1)) * 2 + t0) * 64) + lane] = acc2[2 * tp + t0][a];
  }
  WAITL0();
  BAR();
  f32x4 f0 = acc2[0][0], f1 = acc2[1][0];
#pragma unroll
  for (int g = 1; g < 4; ++g) {
    f0 += red[((((mh * 4 + kq) * 3 + (g - 1)) * 2 + 0) * 64) + lane];
    f1 += red[((((mh * 4 + kq) * 3 + (g - 1)) * 2 + 1) * 64) + lane];
  }
  {
    unsigned short* op = out + (size_t)(rb * 64 + 16 * (2 * mh + (kq >> 1)) + (lane & 15)) * D_ + n0 + 32 * (kq & 1) + 4 * q;
    *(u32x2*)op = u32x2{pack_bf16(f0[0], f0[1]), pack_bf16(f0[2], f0[3])};
    *(u32x2*)(op + 16) = u32x2{pack_bf16(f1[0], f1[1]), pack_bf16(f1[2], f1[3])};
  }
  STAMP(16);
}

extern "C" void kernel_launch(void* const* d_in, const int* in_sizes, int n_in, void* d_out, int out_size, void* d_ws,
                              size_t ws_size, hipStream_t stream) {
  const unsigned short* x = (const unsigned short*)d_in[0];
  const unsigned short* wg = (const unsigned short*)d_in[1];
  const unsigned short* wu = (const unsigned short*)d_in[2];
  const unsigned short* wd = (const unsigned short*)d_in[3];
  (void)hipFuncSetAttribute((const void*)ffn_fused, hipFuncAttributeMaxDynamicSharedMemorySize, LDS_BYTES);
  ffn_fused<<<256, 512, LDS_BYTES, stream>>>(x, wg, wu, wd, (unsigned char*)d_ws, (unsigned short*)d_out);
}
```

```cpp
#include <hip/hip_runtime.h>
#include <hip/hip_bf16.h>

typedef short s16x8 __attribute__((ext_vector_type(8)));
typedef short s16x4 __attribute__((ext_vector_type(4)));
typedef __bf16 bf16x8 __attribute__((ext_vector_type(8)));
typedef float f32x2 __attribute__((ext_vector_type(2)));
typedef float f32x4 __attribute__((ext_vector_type(4)));
typedef float f32x16 __attribute__((ext_vector_type(16)));
typedef unsigned u32x4 __attribute__((ext_vector_type(4)));
typedef unsigned u32x2 __attribute__((ext_vector_type(2)));
#define LAS __attribute__((address_space(3)))
#define GAS __attribute__((address_space(1)))

constexpr int M_ = 2048, D_ = 512, F_ = 1376;
constexpr int NS2 = F_ / 32;
constexpr int SLOT = 40960, STG_X = 16384, STG_W = 12288;
constexpr int LDS_BYTES = 163840;
constexpr unsigned CB_PANEL = 8192, CB_N = 0, CB_FLAG = 512, CB_E = 2048, CB_V = 4096;
constexpr unsigned H_OFF = 16 * CB_PANEL;
constexpr unsigned H_BYTES = M_ * F_ * 2;
constexpr unsigned OOB = 0x40000000u;
constexpr int AUX_SC1 = 16;

#define STAMPS 0
#define OPT_TOUCH 0
#define OPT_B2 0
#define OPT_WIN 4
#define STAMP(i)

__device__ __forceinline__ unsigned pack_bf16(float lo, float hi) {
  typedef __bf16 bf16x2 __attribute__((ext_vector_type(2)));
  f32x2 v = {lo, hi};
  bf16x2 b = __builtin_convertvector(v, bf16x2);
  return __builtin_bit_cast(unsigned, b);
}

#define GLDS(M0V, VOFF, RS, SOFF)                                                                    \
  asm volatile("s_mov_b32 m0, %0\n\ts_nop 0\n\tbuffer_load_dwordx4 %1, %2, %3 offen lds" ::"s"(M0V), \
               "v"(VOFF), "s"(RS), "s"(SOFF)                                                         \
               : "memory")
#define WAITV(n) asm volatile("s_waitcnt vmcnt(" #n ")" ::: "memory")
#define WAITL0() asm volatile("s_waitcnt lgkmcnt(0)" ::: "memory")
#define BAR()                              \
  do {                                     \
    asm volatile("" ::: "memory");         \
    __builtin_amdgcn_s_barrier();          \
    asm volatile("" ::: "memory");         \
  } while (0)
#define SCHED() __builtin_amdgcn_sched_barrier(0)

__device__ __forceinline__ u32x4 swiglu8(const f32x16& c) {
  u32x4 o;
#pragma unroll
  for (int p = 0; p < 4; ++p) {
    f32x2 g = {c[2 * p], c[2 * p + 1]};
    f32x2 u = {c[8 + 2 * p], c[8 + 2 * p + 1]};
    f32x2 t = g * -1.4426950408889634f;
    f32x2 e = {__builtin_amdgcn_exp2f(t[0]), __builtin_amdgcn_exp2f(t[1])};
    e = e + 1.0f;
    f32x2 r = {__builtin_amdgcn_rcpf(e[0]), __builtin_amdgcn_rcpf(e[1])};
    f32x2 h = (g * u) * r;
    o[p] = pack_bf16(h[0], h[1]);
  }
  return o;
}

__global__ __launch_bounds__(512, 2) void ffn_fused(const unsigned short* __restrict__ x,
                                                     const unsigned short* __restrict__ wg,
                                                     const unsigned short* __restrict__ wu,
                                                     const unsigned short* __restrict__ wd,
                                                     unsigned char* __restrict__ ws,
                                                     unsigned short* __restrict__ out) {
  extern __shared__ __attribute__((aligned(16))) unsigned char smem[];
  STAMP(0);
  const int tid = threadIdx.x, lane = tid & 63;
  const int w = __builtin_amdgcn_readfirstlane(tid >> 6);
  const int b = blockIdx.x;
  const int xcd = b & 7, idx = b >> 3;
  const int pl = idx & 3, jl = idx >> 2;
  const int p = 4 * (xcd >> 1) + pl;
  const int j = 8 * (xcd & 1) + jl;
  const int u0 = j < 6 ? 6 * j : 36 + 5 * (j - 6);
  const int nu = j < 6 ? 6 : 5;
  const int m0 = p * 128;
  const int rho = (jl + 2 * pl) & 7;
  const unsigned ldsb = (unsigned)(size_t)(LAS unsigned char*)smem;
  const LAS unsigned char* L = (const LAS unsigned char*)smem;

  __amdgpu_buffer_rsrc_t rh = __builtin_amdgcn_make_buffer_rsrc((void*)(ws + H_OFF), 0, H_BYTES, 0x00020000);
  GAS unsigned char* cb = (GAS unsigned char*)ws + p * CB_PANEL;

  const int kq = w & 3;
  const int mh = w >> 2;
  const int rb = 2 * p + (j >> 3);
  const int n0 = (j & 7) * 64;
  const int q = lane >> 4;

  if (w >= 4) {
    const int lw = w - 4;
    __amdgpu_buffer_rsrc_t rx = __builtin_amdgcn_make_buffer_rsrc((void*)x, 0, M_ * D_ * 2, 0x00020000);
    __amdgpu_buffer_rsrc_t rg = __builtin_amdgcn_make_buffer_rsrc((void*)wg, 0, D_ * F_ * 2, 0x00020000);
    __amdgpu_buffer_rsrc_t ru = __builtin_amdgcn_make_buffer_rsrc((void*)wu, 0, D_ * F_ * 2, 0x00020000);
    __amdgpu_buffer_rsrc_t rd = __builtin_amdgcn_make_buffer_rsrc((void*)wd, 0, F_ * D_ * 2, 0x00020000);
    int xoff[4];
#pragma unroll
    for (int i = 0; i < 4; ++i) {
      const int row = (lw * 4 + i) * 8 + (lane >> 3);
      const int ch = (lane & 7) ^ ((row >> 1) & 7);
      xoff[i] = (m0 + row) * (D_ * 2) + ch * 16;
    }
    int goff[3], uoff[3];
#pragma unroll
    for (int i = 0; i < 3; ++i) {
      const int c = (lw * 3 + i) * 64 + lane;
      const int k = c / 12, cc = c % 12;
      const int seg = cc >> 1, half = cc & 1;
      const int sg_ = seg, su_ = seg ^ 1;
      goff[i] = sg_ < nu ? k * (F_ * 2) + (u0 + sg_) * 32 + half * 16 : (int)OOB;
      uoff[i] = su_ < nu ? k * (F_ * 2) + (u0 + su_) * 32 + half * 16 : (int)OOB;
    }
    const unsigned m0x = ldsb + lw * 4096, m0g = ldsb + STG_X + lw * 3072, m0u = m0g + STG_W;
    auto issue = [&](int pos) {
      const unsigned sb = (pos % 3) * SLOT;
      const int st = (pos + rho) & 7;
      const unsigned sx = st * 128, sw = st * (64 * F_ * 2);
#pragma unroll
      for (int i = 0; i < 4; ++i) GLDS(m0x + sb + i * 1024, xoff[i], rx, sx);
#pragma unroll
      for (int i = 0; i < 3; ++i) GLDS(m0g + sb + i * 1024, goff[i], rg, sw);
#pragma unroll
      for (int i = 0; i < 3; ++i) GLDS(m0u + sb + i * 1024, uoff[i], ru, sw);
    };
    const int drow = lane >> 3;
    const int doff = (8 * lw + drow) * (D_ * 2) + n0 * 2 + ((((lane & 7) >> 1) ^ ((drow >> 1) & 3)) * 32) + (lane & 1) * 16;
    auto issue_wd = [&](int ridx, int sg) {
      const unsigned off = (ridx < 10 ? ridx + 30 : ridx - 10) * 4096;
      GLDS(ldsb + off + lw * 1024, doff, rd, (unsigned)sg * (32 * D_ * 2));
    };

    issue(0);
    issue(1);
    WAITV(10);
    BAR();
#pragma unroll
    for (int k = 0; k < 6; ++k) {
      issue(k + 2);
      WAITV(10);
      BAR();
    }
#pragma unroll
    for (int r = 0; r < 10; ++r) issue_wd(r, 11 * (r & 3) + (r >> 2));
    WAITV(10);
    BAR();
    BAR();
#pragma unroll 1
    for (int i = 0; i < 30; ++i) {
      int r = i + (pl * 30) / 4;
      r = (r >= 30 ? r - 30 : r) + 10;
      issue_wd(r, 11 * (r & 3) + (r >> 2));
    }
    WAITV(30);
    BAR();
  } else {
    const int wm = w >> 1, wf = w & 1;
    const int hh = lane >> 5;
    const int xsw = ((lane & 31) >> 1) & 7;
    const int xrow = (wm * 64 + (lane & 31)) * 128;
    int xk[4];
#pragma unroll
    for (int ks = 0; ks < 4; ++ks) xk[ks] = xrow + (((2 * ks + hh) ^ xsw) * 16);
    const int bsel = (lane >> 4) & 1;
    const int wbase = STG_X + bsel * STG_W + (8 * hh + ((lane >> 2) & 3)) * 192 + (lane & 3) * 8;
    int wa[3];
#pragma unroll
    for (int u = 0; u < 3; ++u) wa[u] = wbase + ((wf * 3 + u) ^ bsel) * 32;

    f32x16 acc[2][3];
#pragma unroll
    for (int a = 0; a < 2; ++a)
#pragma unroll
      for (int u = 0; u < 3; ++u)
#pragma unroll
        for (int r = 0; r < 16; ++r) acc[a][u][r] = 0.f;

    auto rdA = [&](int sb, int ks, int u) -> s16x8 {
      const LAS unsigned char* pp = L + sb + wa[u] + ks * (16 * 192);
      s16x4 lo = __builtin_amdgcn_ds_read_tr16_b64_v4i16((LAS s16x4*)pp);
      s16x4 hi = __builtin_amdgcn_ds_read_tr16_b64_v4i16((LAS s16x4*)(pp + 4 * 192));
      return __builtin_shufflevector(lo, hi, 0, 1, 2, 3, 4, 5, 6, 7);
    };
    auto rdB = [&](int sb, int ks, int a) -> s16x8 { return *(const LAS s16x8*)(L + sb + xk[ks] + a * 4096); };
#define MFMA32(A, B, C) __builtin_amdgcn_mfma_f32_32x32x16_bf16(__builtin_bit_cast(bf16x8, A), __builtin_bit_cast(bf16x8, B), C, 0, 0, 0)

    __amdgpu_buffer_rsrc_t rc = __builtin_amdgcn_make_buffer_rsrc((void*)cb, 0, CB_PANEL, 0x00020000);
    int goffs = (int)OOB, eoffs = (int)OOB, noffs = (int)OOB;
    if (w == 0) {
      if (lane < 16) { goffs = CB_E + j * 128 + lane * 8; eoffs = CB_E + lane * 128 + j * 8; }
      else if (lane < 32) goffs = CB_N + (lane - 16) * 8;
      else if (lane < 36) goffs = CB_FLAG + (lane - 32) * 128 + j * 8;
      if (lane == 0) noffs = CB_N + j * 8;
    }
    unsigned myn, pn = 0;
    bool ver = false;
    auto hs_load = [&]() -> u32x2 { return __builtin_bit_cast(u32x2, __builtin_amdgcn_raw_buffer_load_b64(rc, goffs, 0, AUX_SC1)); };
    auto hs_round = [&](const u32x2& hv) {
      const unsigned nrow = __shfl(hv[0], (lane & 15) + 16);
      const bool nv_ = !ver && hv[0] == myn;
      pn = nv_ ? hv[1] : pn;
      ver = ver || nv_;
      const u32x2 val = {ver ? pn : nrow, myn};
      __builtin_amdgcn_raw_buffer_store_b64(val, rc, eoffs, 0, AUX_SC1);
    };
    u32x2 hv = hs_load();
    {
      unsigned c = __shfl(hv[0], 16 + j);
      const bool mine = lane < 16 || (lane >= 32 && lane < 36);
#pragma unroll 1
      do { ++c; } while (__any(mine && (hv[0] == c || hv[1] == c)));
      myn = c;
      __builtin_amdgcn_raw_buffer_store_b64(u32x2{myn, myn}, rc, noffs, 0, AUX_SC1);
    }
    asm volatile("" ::: "memory");
    hv = hs_load();

    s16x8 fa[2][3], fb[2][2];
    unsigned ownn = 0;
    BAR();
    STAMP(1);
#pragma unroll
    for (int u = 0; u < 3; ++u) fa[0][u] = rdA(0, 0, u);
#pragma unroll
    for (int a = 0; a < 2; ++a) fb[0][a] = rdB(0, 0, a);
#pragma unroll
    for (int g = 0; g < 28; ++g) {
      const int pos = g >> 2, ks = g & 3;
      if (ks == 3) {
        SCHED();
        WAITL0();
        BAR();
        STAMP(2 + pos);
        if (pos == 2)
          ownn = __hip_atomic_load((GAS unsigned*)(cb + CB_N + j * 8), __ATOMIC_RELAXED, __HIP_MEMORY_SCOPE_AGENT);
        if (!(pos & 1)) {
          asm volatile("" : "+v"(hv));
          hs_round(hv);
          if (pos < 6) hv = hs_load();
        }
      }
      SCHED();
      const int cur = g & 1, nxt = cur ^ 1;
      if (g + 1 < 28) {
        const int npos = (g + 1) >> 2, nks = (g + 1) & 3, sb = (npos % 3) * SLOT;
        fb[nxt][0] = rdB(sb, nks, 0);
#pragma unroll
        for (int u = 0; u < 3; ++u) fa[nxt][u] = rdA(sb, nks, u);
        fb[nxt][1] = rdB(sb, nks, 1);
#pragma unroll
        for (int a = 0; a < 2; ++a)
#pragma unroll
          for (int u = 0; u < 3; ++u) acc[a][u] = MFMA32(fa[cur][u], fb[cur][a], acc[a][u]);
#pragma unroll
        for (int i = 0; i < 4; ++i) {
          __builtin_amdgcn_sched_group_barrier(0x8, 1, 0);
          __builtin_amdgcn_sched_group_barrier(0x100, 2, 0);
        }
        __builtin_amdgcn_sched_group_barrier(0x8, 2, 0);
      }
    }
    s16x8 la[3][4], lb[2][4];
    {
      constexpr int sb = (7 % 3) * SLOT;
#pragma unroll
      for (int ks = 0; ks < 4; ++ks) {
        la[0][ks] = rdA(sb, ks, 0);
        lb[0][ks] = rdB(sb, ks, 0);
      }
#pragma unroll
      for (int a = 0; a < 2; ++a)
#pragma unroll
        for (int u = 0; u < 3; ++u) acc[a][u] = MFMA32(fa[1][u], fb[1][a], acc[a][u]);
#pragma unroll
      for (int i = 0; i < 6; ++i) {
        __builtin_amdgcn_sched_group_barrier(0x8, 1, 0);
        __builtin_amdgcn_sched_group_barrier(0x100, 2, 0);
      }
      SCHED();
#pragma unroll
      for (int ks = 0; ks < 4; ++ks) la[1][ks] = rdA(sb, ks, 1);
#pragma unroll
      for (int ks = 0; ks < 4; ++ks) acc[0][0] = MFMA32(la[0][ks], lb[0][ks], acc[0][0]);
#pragma unroll
      for (int i = 0; i < 4; ++i) {
        __builtin_amdgcn_sched_group_barrier(0x100, 2, 0);
        __builtin_amdgcn_sched_group_barrier(0x8, 1, 0);
      }
      SCHED();
#pragma unroll
      for (int ks = 0; ks < 4; ++ks) la[2][ks] = rdA(sb, ks, 2);
#pragma unroll
      for (int ks = 0; ks < 4; ++ks) lb[1][ks] = rdB(sb, ks, 1);
    }
    const int ugb = u0 + wf * 3;
    const int nuw = wf ? nu - 3 : 3;
    auto hstore = [&](int a, int u, u32x4 v) {
      const int m = m0 + wm * 64 + a * 32 + (lane & 31);
      const int ug = ugb + u;
      const int voff = ((m >> 4) * NS2 * 64 + (m & 15) + 16 * hh) * 16;
      const unsigned soff = u < nuw ? (unsigned)((ug >> 1) * 1024 + (ug & 1) * 512) : OOB;
      __builtin_amdgcn_raw_buffer_store_b128(v, rh, voff + (int)soff, 0, AUX_SC1);
    };
#pragma unroll
    for (int ks = 0; ks < 4; ++ks) acc[0][1] = MFMA32(la[1][ks], lb[0][ks], acc[0][1]);
    hstore(0, 0, swiglu8(acc[0][0]));
    SCHED();
    WAITL0();
    BAR();
    STAMP(9);
    SCHED();
#pragma unroll
    for (int ks = 0; ks < 4; ++ks) acc[0][2] = MFMA32(la[2][ks], lb[0][ks], acc[0][2]);
    hstore(0, 1, swiglu8(acc[0][1]));
    SCHED();
#pragma unroll
    for (int ks = 0; ks < 4; ++ks) acc[1][0] = MFMA32(la[0][ks], lb[1][ks], acc[1][0]);
    hstore(0, 2, swiglu8(acc[0][2]));
    SCHED();
#pragma unroll
    for (int ks = 0; ks < 4; ++ks) acc[1][1] = MFMA32(la[1][ks], lb[1][ks], acc[1][1]);
    hstore(1, 0, swiglu8(acc[1][0]));
    SCHED();
#pragma unroll
    for (int ks = 0; ks < 4; ++ks) acc[1][2] = MFMA32(la[2][ks], lb[1][ks], acc[1][2]);
    hstore(1, 1, swiglu8(acc[1][1]));
    SCHED();
    GAS unsigned* fl = (GAS unsigned*)(cb + CB_FLAG) + (wm * 2) * 32 + j * 2 + wf;
    WAITV(2);
    if (lane == 0) __hip_atomic_store(fl, ownn, __ATOMIC_RELAXED, __HIP_MEMORY_SCOPE_AGENT);
    SCHED();
    hstore(1, 2, swiglu8(acc[1][2]));
    STAMP(10);
    if (w == 0) {
#pragma unroll 1
      for (int spin = 0; spin < (1 << 16) && !__all(lane >= 16 || ver); ++spin) {
        __builtin_amdgcn_s_sleep(1);
        asm volatile("" ::: "memory");
        hv = hs_load();
        hs_round(hv);
      }
      if (lane < 16) __hip_atomic_store((GAS unsigned*)(cb + CB_V + j * 128 + lane * 4), pn, __ATOMIC_RELAXED, __HIP_MEMORY_SCOPE_AGENT);
    }
    WAITV(0);
    if (lane == 0) __hip_atomic_store(fl + 32, ownn, __ATOMIC_RELAXED, __HIP_MEMORY_SCOPE_AGENT);
    STAMP(11);
    BAR();
  }

  int aoff[4];
  {
    const int qp = (lane >> 2) & 3, pp = lane & 3;
    const int row = 16 * (q >> 1) + 4 * (q & 1) + qp;
    const int g = 2 * (q & 1) + (qp >> 1);
#pragma unroll
    for (int t = 0; t < 4; ++t) {
      const int nt = t ^ (2 * (kq & 1));
      aoff[t] = row * 128 + ((nt ^ g) * 32) + pp * 8;
    }
  }
  auto rdW = [&](int roff, int t) -> s16x8 {
    const LAS unsigned char* pp = L + roff + aoff[t];
    s16x4 lo = __builtin_amdgcn_ds_read_tr16_b64_v4i16((LAS s16x4*)pp);
    s16x4 hi = __builtin_amdgcn_ds_read_tr16_b64_v4i16((LAS s16x4*)(pp + 1024));
    return __builtin_shufflevector(lo, hi, 0, 1, 2, 3, 4, 5, 6, 7);
  };
  const int roff0 = (30 + kq) * 4096;
  s16x8 a0[4];
#pragma unroll
  for (int t = 0; t < 4; ++t) a0[t] = rdW(roff0, t);
  WAITL0();
  BAR();
  STAMP(12);

  const int pi = (lane >> 1) & 15, pwf = lane & 1;
  const int pu0 = (pi < 6 ? 6 * pi : 36 + 5 * (pi - 6)) + 3 * pwf;
  const int pue = pwf ? (pi < 6 ? 6 * pi + 6 : 36 + 5 * (pi - 6) + 5) : pu0 + 3;
  const bool need = lane < 32 && pu0 < 22 * kq + 22 && pue > 22 * kq;
  GAS unsigned* pf = (GAS unsigned*)(cb + CB_FLAG) + ((j >> 3) * 2) * 32 + (lane & 31);
  auto poll = [&](GAS unsigned* f, unsigned want) {
#pragma unroll 1
    for (int spin = 0; spin < (1 << 16); ++spin) {
      const unsigned v = __hip_atomic_load(f, __ATOMIC_RELAXED, __HIP_MEMORY_SCOPE_AGENT);
      if (__all(!need || v == want)) break;
      __builtin_amdgcn_s_sleep(1);
    }
    __builtin_amdgcn_fence(__ATOMIC_ACQUIRE, "wavefront");
  };
  const int rot = ((j & 7) * 9) >> 3;
  u32x4 hb[11][2];
  auto hloads = [&]() {
#pragma unroll
    for (int i = 0; i < 11; ++i) {
      int rel;
      if (i == 0) rel = 0;
      else if (i == 10) rel = 10;
      else { rel = i - 1 + rot; rel = (rel >= 9 ? rel - 9 : rel) + 1; }
      const int sg = 11 * kq + rel;
#pragma unroll
      for (int a = 0; a < 2; ++a) {
        const int mt = 4 * rb + 2 * mh + (a ^ (kq >> 1));
        unsigned so = (unsigned)(mt * NS2 + sg) * 1024;
        if (i == 10) so |= (unsigned)(kq == 3) << 30;
        hb[i][a] = __builtin_bit_cast(u32x4, __builtin_amdgcn_raw_buffer_load_b128(rh, lane * 16, so, AUX_SC1));
      }
    }
    SCHED();
  };

  const unsigned want = __hip_atomic_load((GAS unsigned*)(cb + CB_V + j * 128 + pi * 4), __ATOMIC_RELAXED, __HIP_MEMORY_SCOPE_AGENT);
  if (w >= 4) {
    const int lw = w - 4;
    __amdgpu_buffer_rsrc_t rd = __builtin_amdgcn_make_buffer_rsrc((void*)wd, 0, F_ * D_ * 2, 0x00020000);
    const int drow = lane >> 3;
    const int doff = (8 * lw + drow) * (D_ * 2) + n0 * 2 + ((((lane & 7) >> 1) ^ ((drow >> 1) & 3)) * 32) + (lane & 1) * 16;
#pragma unroll
    for (int r = 0; r < 3; ++r) GLDS(ldsb + (30 + r) * 4096 + lw * 1024, doff, rd, (unsigned)(11 * r + 10) * (32 * D_ * 2));
    WAITV(0);
    BAR();
  }
  poll(pf + mh * 32, want);
  STAMP(13);
  hloads();
  if (w < 4) BAR();
  STAMP(14);

  f32x4 acc2[4][2];
#pragma unroll
  for (int t = 0; t < 4; ++t)
#pragma unroll
    for (int a = 0; a < 2; ++a) acc2[t][a] = f32x4{0.f, 0.f, 0.f, 0.f};
#define MFMA16(A, B, C) __builtin_amdgcn_mfma_f32_16x16x32_bf16(__builtin_bit_cast(bf16x8, A), __builtin_bit_cast(bf16x8, B), C, 0, 0, 0)
  s16x8 af[2][4];
#pragma unroll
  for (int t = 0; t < 4; ++t) af[0][t] = a0[t];
#pragma unroll
  for (int i = 0; i < 11; ++i) {
    const int cur = i & 1, nxt = cur ^ 1;
    if (i + 1 < 11) {
      int roff;
      if (i + 1 == 10) roff = roff0;
      else {
        int rel = i + rot;
        rel = (rel >= 9 ? rel - 9 : rel) + 1;
        const int ridx = rel * 4 + kq;
        roff = (ridx < 10 ? ridx + 30 : ridx - 10) * 4096;
      }
#pragma unroll
      for (int t = 0; t < 4; ++t) af[nxt][t] = rdW(roff, t);
    }
#pragma unroll
    for (int a = 0; a < 2; ++a)
#pragma unroll
      for (int t = 0; t < 4; ++t) acc2[t][a] = MFMA16(af[cur][t], hb[i][a], acc2[t][a]);
    if (i + 1 < 11) {
#pragma unroll
      for (int z = 0; z < 4; ++z) {
        __builtin_amdgcn_sched_group_barrier(0x100, 2, 1);
        __builtin_amdgcn_sched_group_barrier(0x8, 1, 1);
      }
      __builtin_amdgcn_sched_group_barrier(0x8, 4, 1);
    }
    SCHED();
  }
  STAMP(15);
  BAR();
  LAS f32x4* red = (LAS f32x4*)smem;
#pragma unroll
  for (int g = 1; g < 4; ++g) {
    const int a = g >> 1, tp = g & 1;
#pragma unroll
    for (int t0 = 0; t0 < 2; ++t0) red[((((mh * 4 + (kq ^ g)) * 3 + (g - 1)) * 2 + t0) * 64) + lane] = acc2[2 * tp + t0][a];
  }
  WAITL0();
  BAR();
  f32x4 f0 = acc2[0][0], f1 = acc2[1][0];
#pragma unroll
  for (int g = 1; g < 4; ++g) {
    f0 += red[((((mh * 4 + kq) * 3 + (g - 1)) * 2 + 0) * 64) + lane];
    f1 += red[((((mh * 4 + kq) * 3 + (g - 1)) * 2 + 1) * 64) + lane];
  }
  {
    unsigned short* op = out + (size_t)(rb * 64 + 16 * (2 * mh + (kq >> 1)) + (lane & 15)) * D_ + n0 + 32 * (kq & 1) + 4 * q;
    *(u32x2*)op = u32x2{pack_bf16(f0[0], f0[1]), pack_bf16(f0[2], f0[3])};
    *(u32x2*)(op + 16) = u32x2{pack_bf16(f1[0], f1[1]), pack_bf16(f1[2], f1[3])};
  }
  STAMP(16);
}

extern "C" void kernel_launch(void* const* d_in, const int* in_sizes, int n_in, void* d_out, int out_size, void* d_ws,
                              size_t ws_size, hipStream_t stream) {
  const unsigned short* x = (const unsigned short*)d_in[0];
  const unsigned short* wg = (const unsigned short*)d_in[1];
  const unsigned short* wu = (const unsigned short*)d_in[2];
  const unsigned short* wd = (const unsigned short*)d_in[3];
  (void)hipFuncSetAttribute((const void*)ffn_fused, hipFuncAttributeMaxDynamicSharedMemorySize, LDS_BYTES);
  ffn_fused<<<256, 512, LDS_BYTES, stream>>>(x, wg, wu, wd, (unsigned char*)d_ws, (unsigned short*)d_out);
}
```

```cpp
#include <hip/hip_runtime.h>
#include <hip/hip_bf16.h>

typedef short s16x8 __attribute__((ext_vector_type(8)));
typedef short s16x4 __attribute__((ext_vector_type(4)));
typedef __bf16 bf16x8 __attribute__((ext_vector_type(8)));
typedef float f32x2 __attribute__((ext_vector_type(2)));
typedef float f32x4 __attribute__((ext_vector_type(4)));
typedef float f32x16 __attribute__((ext_vector_type(16)));
typedef unsigned u32x4 __attribute__((ext_vector_type(4)));
typedef unsigned u32x2 __attribute__((ext_vector_type(2)));
#define LAS __attribute__((address_space(3)))
#define GAS __attribute__((address_space(1)))

constexpr int M_ = 2048, D_ = 512, F_ = 1376;
constexpr int NS2 = F_ / 32;
constexpr int SLOT = 40960, STG_X = 16384, STG_W = 12288;
constexpr int LDS_BYTES = 163840;
constexpr int LDS_CTRL = 159744;
constexpr unsigned CB_PANEL = 8192, CB_N = 0, CB_FLAG = 512, CB_E = 2048, CB_V = 4096;
constexpr unsigned H_OFF = 16 * CB_PANEL;
constexpr unsigned H_BYTES = M_ * F_ * 2;
constexpr unsigned OOB = 0x40000000u;
constexpr int AUX_SC1 = 16;

#define STAMPS 0
#define OPT_TOUCH 0
#define OPT_B2 0
#define OPT_RMASK 4
#define OPT_HAUX AUX_SC1
#define OPT_SAUX AUX_SC1
#define OPT_HD 3
#define OPT_HROT 1
#define OPT_HSR 0x48
#define OPT_EARLY 1
#define OPT_E 6
#define OPT_WIN 4
#define STAMP(i)

__device__ __forceinline__ unsigned pack_bf16(float lo, float hi) {
  typedef __bf16 bf16x2 __attribute__((ext_vector_type(2)));
  f32x2 v = {lo, hi};
  bf16x2 b = __builtin_convertvector(v, bf16x2);
  return __builtin_bit_cast(unsigned, b);
}

#define GLDS(M0V, VOFF, RS, SOFF)                                                                    \
  asm volatile("s_mov_b32 m0, %0\n\ts_nop 0\n\tbuffer_load_dwordx4 %1, %2, %3 offen lds" ::"s"(M0V), \
               "v"(VOFF), "s"(RS), "s"(SOFF)                                                         \
               : "memory")
#define WAITV(n) asm volatile("s_waitcnt vmcnt(" #n ")" ::: "memory")
#define WAITL0() asm volatile("s_waitcnt lgkmcnt(0)" ::: "memory")
#define BAR()                              \
  do {                                     \
    asm volatile("" ::: "memory");         \
    __builtin_amdgcn_s_barrier();          \
    asm volatile("" ::: "memory");         \
  } while (0)
#define SCHED() __builtin_amdgcn_sched_barrier(0)

__device__ __forceinline__ u32x4 swiglu8(const f32x16& c) {
  u32x4 o;
#pragma unroll
  for (int p = 0; p < 4; ++p) {
    f32x2 g = {c[2 * p], c[2 * p + 1]};
    f32x2 u = {c[8 + 2 * p], c[8 + 2 * p + 1]};
    f32x2 t = g * -1.4426950408889634f;
    f32x2 e = {__builtin_amdgcn_exp2f(t[0]), __builtin_amdgcn_exp2f(t[1])};
    e = e + 1.0f;
    f32x2 r = {__builtin_amdgcn_rcpf(e[0]), __builtin_amdgcn_rcpf(e[1])};
    f32x2 h = (g * u) * r;
    o[p] = pack_bf16(h[0], h[1]);
  }
  return o;
}

__global__ __launch_bounds__(512, 2) void ffn_fused(const unsigned short* __restrict__ x,
                                                     const unsigned short* __restrict__ wg,
                                                     const unsigned short* __restrict__ wu,
                                                     const unsigned short* __restrict__ wd,
                                                     unsigned char* __restrict__ ws,
                                                     unsigned short* __restrict__ out) {
  extern __shared__ __attribute__((aligned(16))) unsigned char smem[];
  asm volatile("" ::"s"(x), "s"(wg), "s"(wu), "s"(wd), "s"(ws), "s"(out));
  STAMP(0);
  const int tid = threadIdx.x, lane = tid & 63;
  const int w = __builtin_amdgcn_readfirstlane(tid >> 6);
  const int b = blockIdx.x;
  const int xcd = b & 7, idx = b >> 3;
  const int pl = idx & 3, jl = idx >> 2;
  const int p = 4 * (xcd >> 1) + pl;
  const int j = 8 * (xcd & 1) + jl;
  const int u0 = j < 6 ? 6 * j : 36 + 5 * (j - 6);
  const int nu = j < 6 ? 6 : 5;
  const int m0 = p * 128;
  const int rho = (jl + 2 * pl) & OPT_RMASK;
  const int wrot = (pl * 39) >> 2;
  const unsigned ldsb = (unsigned)(size_t)(LAS unsigned char*)smem;
  const LAS unsigned char* L = (const LAS unsigned char*)smem;

  __amdgpu_buffer_rsrc_t rh = __builtin_amdgcn_make_buffer_rsrc((void*)(ws + H_OFF), 0, H_BYTES, 0x00020000);
  GAS unsigned char* cb = (GAS unsigned char*)ws + p * CB_PANEL;

  const int kq = w & 3;
  const int mh = w >> 2;
  const int rb = 2 * p + (j >> 3);
  const int n0 = (j & 7) * 64;
  const int q = lane >> 4;

  auto mk_aoff = [&](int lane_) -> int {
    const int q_ = lane_ >> 4, qp = (lane_ >> 2) & 3, pp = lane_ & 3;
    const int row = 16 * (q_ >> 1) + 4 * (q_ & 1) + qp;
    const int g = 2 * (q_ & 1) + (qp >> 1);
    return row * 128 + (((2 * (kq & 1)) ^ g) * 32) + pp * 8;
  };
  auto rdW = [&](int roff, int aoff, int t) -> s16x8 {
    const LAS unsigned char* pp = L + roff + (aoff ^ (t << 5));
    s16x4 lo = __builtin_amdgcn_ds_read_tr16_b64_v4i16((LAS s16x4*)pp);
    s16x4 hi = __builtin_amdgcn_ds_read_tr16_b64_v4i16((LAS s16x4*)(pp + 1024));
    return __builtin_shufflevector(lo, hi, 0, 1, 2, 3, 4, 5, 6, 7);
  };
  auto opaque_lane = [&]() -> int { int l = tid & 63; asm volatile("" : "+v"(l)); return l; };
  unsigned pn_out = 0;
  s16x8 a0[4];
  volatile LAS unsigned* ctrl = (volatile LAS unsigned*)(smem + LDS_CTRL);

  if (__builtin_expect(w >= 4, 1)) {
    const int lw = w - 4;
    STAMP(1);
    __amdgpu_buffer_rsrc_t rx = __builtin_amdgcn_make_buffer_rsrc((void*)x, 0, M_ * D_ * 2, 0x00020000);
    __amdgpu_buffer_rsrc_t rg = __builtin_amdgcn_make_buffer_rsrc((void*)wg, 0, D_ * F_ * 2, 0x00020000);
    __amdgpu_buffer_rsrc_t ru = __builtin_amdgcn_make_buffer_rsrc((void*)wu, 0, D_ * F_ * 2, 0x00020000);
    __amdgpu_buffer_rsrc_t rd = __builtin_amdgcn_make_buffer_rsrc((void*)wd, 0, F_ * D_ * 2, 0x00020000);
    const int xoE = (lane >> 3) * 1024 + (((lane & 7) ^ ((lane >> 4) & 3)) * 16), xoO = xoE ^ 64;
    const unsigned xs0 = (unsigned)(m0 + 32 * lw) * 1024;
    const unsigned m0x = ldsb + lw * 4096, m0g = ldsb + STG_X + lw * 3072, m0u = m0g + STG_W;
    {
      const unsigned sx = xs0 + rho * 128;
      GLDS(m0x, xoE, rx, sx);
      GLDS(m0x + 1024, xoO, rx, sx + 8192);
      GLDS(m0x + 2048, xoE, rx, sx + 16384);
      GLDS(m0x + 3072, xoO, rx, sx + 24576);
    }
    int goff[3], uoff[3];
    const int ud = (lane & 2) ? -32 : 32;
#pragma unroll
    for (int i = 0; i < 3; ++i) {
      const int t = 64 * i + lane;
      const int k = (int)(__umul24(t, 171) >> 11);
      const int cc = t - 12 * k;
      const int g0 = k * 2560 + 16 * t;
      goff[i] = (nu == 5 && cc >= 10) ? (int)OOB : g0;
      uoff[i] = (nu == 5 && (cc >> 1) == 4) ? (int)OOB : g0 + ud;
    }
    const unsigned ws0 = (unsigned)(16 * lw) * (F_ * 2) + u0 * 32;
    auto issue = [&](int pos) {
      const unsigned sb = (pos % 3) * SLOT;
      const int st = (pos + rho) & 7;
      const unsigned sx = xs0 + st * 128, sw = ws0 + st * (64 * F_ * 2);
      if (pos > 0) {
        GLDS(m0x + sb, xoE, rx, sx);
        GLDS(m0x + sb + 1024, xoO, rx, sx + 8192);
        GLDS(m0x + sb + 2048, xoE, rx, sx + 16384);
        GLDS(m0x + sb + 3072, xoO, rx, sx + 24576);
      }
#pragma unroll
      for (int i = 0; i < 3; ++i) GLDS(m0g + sb + i * 1024, goff[i], rg, sw);
#pragma unroll
      for (int i = 0; i < 3; ++i) GLDS(m0u + sb + i * 1024, uoff[i], ru, sw);
    };
    const int drow = lane >> 3;
    const int doff = (8 * lw + drow) * (D_ * 2) + n0 * 2 + ((((lane & 7) >> 1) ^ ((drow >> 1) & 3)) * 32) + (lane & 1) * 16;
    auto issue_wd = [&](int o) {
      int ridx = o + wrot;
      ridx = ridx >= 39 ? ridx - 39 : ridx;
      const unsigned off = o < 9 ? (30 + o) * 4096 : (o < 19 ? (o + 11) * 4096 : (o - 19) * 4096);
      GLDS(ldsb + off + lw * 1024, doff, rd, (unsigned)(11 * (ridx & 3) + (ridx >> 2)) * (32 * D_ * 2));
    };
    STAMP(2);
    issue(0);
    STAMP(3);
    issue(1);
    STAMP(4);
    WAITV(10);
    STAMP(5);
    BAR();
    STAMP(6);
    issue(2);
    WAITV(10);
    BAR();
#pragma unroll
    for (int r = 0; r < 4; ++r) GLDS(ldsb + (30 + r) * 4096 + lw * 1024, doff, rd, (unsigned)(r < 3 ? 11 * r + 10 : 42) * (32 * D_ * 2));
    issue(3);
    WAITV(14);
    BAR();
    issue(4);
    WAITV(10);
    BAR();
    {
      const int ao = mk_aoff(opaque_lane());
#pragma unroll
      for (int t = 0; t < 4; ++t) a0[t] = rdW((30 + kq) * 4096, ao, t);
    }
    issue(5);
    WAITV(10);
    WAITL0();
    BAR();
    issue(6);
#pragma unroll
    for (int r = 0; r < 3; ++r) issue_wd(r);
    WAITV(13);
    BAR();
    issue(7);
#pragma unroll
    for (int r = 3; r < 6; ++r) issue_wd(r);
    WAITV(16);
    BAR();
#pragma unroll
    for (int r = 6; r < 9 + OPT_E; ++r) issue_wd(r);
    asm volatile("s_waitcnt vmcnt(%0)" ::"i"(6 + OPT_E) : "memory");
    BAR();
    BAR();
#pragma unroll 1
    for (int o = 9 + OPT_E; o < 39; ++o) issue_wd(o);
    WAITV(0);
    if (lane == 0) __hip_atomic_fetch_add((LAS unsigned*)ctrl, 1u, __ATOMIC_RELAXED, __HIP_MEMORY_SCOPE_WORKGROUP);
  } else {
    const int wm = w >> 1, wf = w & 1;
    const int hh = lane >> 5;
    const int xsw = ((lane & 31) >> 1) & 7;
    const int xrow = (wm * 64 + (lane & 31)) * 128;
    int xk[4];
#pragma unroll
    for (int ks = 0; ks < 4; ++ks) xk[ks] = xrow + (((2 * ks + hh) ^ xsw) * 16);
    const int bsel = (lane >> 4) & 1;
    const int wbase = STG_X + bsel * STG_W + (8 * hh + ((lane >> 2) & 3)) * 192 + (lane & 3) * 8;
    int wa[3];
#pragma unroll
    for (int u = 0; u < 3; ++u) wa[u] = wbase + ((wf * 3 + u) ^ bsel) * 32;

    f32x16 acc[2][3];
#pragma unroll
    for (int a = 0; a < 2; ++a)
#pragma unroll
      for (int u = 0; u < 3; ++u)
#pragma unroll
        for (int r = 0; r < 16; ++r) acc[a][u][r] = 0.f;

    auto rdA = [&](int sb, int ks, int u) -> s16x8 {
      const LAS unsigned char* pp = L + sb + wa[u] + ks * (16 * 192);
      s16x4 lo = __builtin_amdgcn_ds_read_tr16_b64_v4i16((LAS s16x4*)pp);
      s16x4 hi = __builtin_amdgcn_ds_read_tr16_b64_v4i16((LAS s16x4*)(pp + 4 * 192));
      return __builtin_shufflevector(lo, hi, 0, 1, 2, 3, 4, 5, 6, 7);
    };
    auto rdB = [&](int sb, int ks, int a) -> s16x8 { return *(const LAS s16x8*)(L + sb + xk[ks] + a * 4096); };
#define MFMA32(A, B, C) __builtin_amdgcn_mfma_f32_32x32x16_bf16(__builtin_bit_cast(bf16x8, A), __builtin_bit_cast(bf16x8, B), C, 0, 0, 0)

    __amdgpu_buffer_rsrc_t rc = __builtin_amdgcn_make_buffer_rsrc((void*)cb, 0, CB_PANEL, 0x00020000);
    int goffs = (int)OOB, eoffs = (int)OOB, noffs = (int)OOB;
    if (w == 0) {
      if (lane < 16) { goffs = CB_E + j * 128 + lane * 8; eoffs = CB_E + lane * 128 + j * 8; }
      else if (lane < 32) goffs = CB_N + (lane - 16) * 8;
      else if (lane < 36) goffs = CB_FLAG + (lane - 32) * 128 + j * 8;
      if (lane == 0) noffs = CB_N + j * 8;
    }
    unsigned myn, pn = 0;
    bool ver = false;
    auto hs_load = [&]() -> u32x2 { return __builtin_bit_cast(u32x2, __builtin_amdgcn_raw_buffer_load_b64(rc, goffs, 0, AUX_SC1)); };
    auto hs_round = [&](const u32x2& hv) {
      const unsigned nrow = __shfl(hv[0], (lane & 15) + 16);
      const bool nv_ = !ver && hv[0] == myn;
      pn = nv_ ? hv[1] : pn;
      ver = ver || nv_;
      const u32x2 val = {ver ? pn : nrow, myn};
      __builtin_amdgcn_raw_buffer_store_b64(val, rc, eoffs, 0, AUX_SC1);
    };
    STAMP(19);
    u32x2 hv = hs_load();
    {
      unsigned c = __shfl(hv[0], 16 + j);
      const bool mine = lane < 16 || (lane >= 32 && lane < 36);
#pragma unroll 1
      do { ++c; } while (__any(mine && (hv[0] == c || hv[1] == c)));
      myn = c;
      __builtin_amdgcn_raw_buffer_store_b64(u32x2{myn, myn}, rc, noffs, 0, AUX_SC1);
    }
    asm volatile("" ::: "memory");
    STAMP(20);

    s16x8 fa[2][3], fb[2][2];
    unsigned ownn = 0;
    if (tid == 64) { ctrl[0] = 0u; ctrl[1] = 0u; }
    WAITL0();
    BAR();
    STAMP(1);
#pragma unroll
    for (int u = 0; u < 3; ++u) fa[0][u] = rdA(0, 0, u);
#pragma unroll
    for (int a = 0; a < 2; ++a) fb[0][a] = rdB(0, 0, a);
#pragma unroll
    for (int g = 0; g < 28; ++g) {
      const int pos = g >> 2, ks = g & 3;
      if (ks == 3) {
        SCHED();
        if (pos == 5)
          ownn = __hip_atomic_load((GAS unsigned*)(cb + CB_N + j * 8), __ATOMIC_RELAXED, __HIP_MEMORY_SCOPE_AGENT);
        if (pos == 0) hv = hs_load();
        if ((OPT_HSR >> pos) & 1) {
          asm volatile("" : "+v"(hv));
          hs_round(hv);
          hv = hs_load();
        }
        if (pos == 3) {
          const int ao = mk_aoff(opaque_lane());
#pragma unroll
          for (int t = 0; t < 4; ++t) a0[t] = rdW((30 + kq) * 4096, ao, t);
        }
        WAITL0();
        BAR();
        STAMP(2 + pos);
      }
      SCHED();
      const int cur = g & 1, nxt = cur ^ 1;
      if (g + 1 < 28) {
        const int npos = (g + 1) >> 2, nks = (g + 1) & 3, sb = (npos % 3) * SLOT;
        fb[nxt][0] = rdB(sb, nks, 0);
#pragma unroll
        for (int u = 0; u < 3; ++u) fa[nxt][u] = rdA(sb, nks, u);
        fb[nxt][1] = rdB(sb, nks, 1);
#pragma unroll
        for (int a = 0; a < 2; ++a)
#pragma unroll
          for (int u = 0; u < 3; ++u) acc[a][u] = MFMA32(fa[cur][u], fb[cur][a], acc[a][u]);
#pragma unroll
        for (int i = 0; i < 4; ++i) {
          __builtin_amdgcn_sched_group_barrier(0x8, 1, 0);
          __builtin_amdgcn_sched_group_barrier(0x100, 2, 0);
        }
        __builtin_amdgcn_sched_group_barrier(0x8, 2, 0);
      }
    }
    s16x8 la[3][4], lb[2][4];
    {
      constexpr int sb = (7 % 3) * SLOT;
#pragma unroll
      for (int ks = 0; ks < 4; ++ks) {
        la[0][ks] = rdA(sb, ks, 0);
        lb[0][ks] = rdB(sb, ks, 0);
      }
#pragma unroll
      for (int a = 0; a < 2; ++a)
#pragma unroll
        for (int u = 0; u < 3; ++u) acc[a][u] = MFMA32(fa[1][u], fb[1][a], acc[a][u]);
#pragma unroll
      for (int i = 0; i < 6; ++i) {
        __builtin_amdgcn_sched_group_barrier(0x8, 1, 0);
        __builtin_amdgcn_sched_group_barrier(0x100, 2, 0);
      }
      SCHED();
#pragma unroll
      for (int ks = 0; ks < 4; ++ks) la[1][ks] = rdA(sb, ks, 1);
#pragma unroll
      for (int ks = 0; ks < 4; ++ks) acc[0][0] = MFMA32(la[0][ks], lb[0][ks], acc[0][0]);
#pragma unroll
      for (int i = 0; i < 4; ++i) {
        __builtin_amdgcn_sched_group_barrier(0x100, 2, 0);
        __builtin_amdgcn_sched_group_barrier(0x8, 1, 0);
      }
      SCHED();
#pragma unroll
      for (int ks = 0; ks < 4; ++ks) la[2][ks] = rdA(sb, ks, 2);
#pragma unroll
      for (int ks = 0; ks < 4; ++ks) lb[1][ks] = rdB(sb, ks, 1);
    }
    const int ugb = u0 + wf * 3;
    const int nuw = wf ? nu - 3 : 3;
    auto hstore = [&](int a, int u, u32x4 v) {
      const int m = m0 + wm * 64 + a * 32 + (lane & 31);
      const int ug = ugb + u;
      const int voff = ((m >> 4) * NS2 * 64 + (m & 15) + 16 * hh) * 16;
      const unsigned soff = u < nuw ? (unsigned)((ug >> 1) * 1024 + (ug & 1) * 512) : OOB;
      __builtin_amdgcn_raw_buffer_store_b128(v, rh, voff + (int)soff, 0, OPT_SAUX);
    };
#pragma unroll
    for (int ks = 0; ks < 4; ++ks) acc[0][1] = MFMA32(la[1][ks], lb[0][ks], acc[0][1]);
    hstore(0, 0, swiglu8(acc[0][0]));
    SCHED();
    if ((OPT_HSR >> 7) & 1) {
      asm volatile("" : "+v"(hv));
      hs_round(hv);
    }
    WAITL0();
    BAR();
    STAMP(9);
    SCHED();
#pragma unroll
    for (int ks = 0; ks < 4; ++ks) acc[0][2] = MFMA32(la[2][ks], lb[0][ks], acc[0][2]);
    hstore(0, 1, swiglu8(acc[0][1]));
    SCHED();
#pragma unroll
    for (int ks = 0; ks < 4; ++ks) acc[1][0] = MFMA32(la[0][ks], lb[1][ks], acc[1][0]);
    hstore(0, 2, swiglu8(acc[0][2]));
    SCHED();
#pragma unroll
    for (int ks = 0; ks < 4; ++ks) acc[1][1] = MFMA32(la[1][ks], lb[1][ks], acc[1][1]);
    hstore(1, 0, swiglu8(acc[1][0]));
    SCHED();
#pragma unroll
    for (int ks = 0; ks < 4; ++ks) acc[1][2] = MFMA32(la[2][ks], lb[1][ks], acc[1][2]);
    hstore(1, 1, swiglu8(acc[1][1]));
    SCHED();
    GAS unsigned* fl = (GAS unsigned*)(cb + CB_FLAG) + (wm * 2) * 32 + j * 2 + wf;
    WAITV(2);
    STAMP(18);
    if (lane == 0) __hip_atomic_store(fl, ownn, __ATOMIC_RELAXED, __HIP_MEMORY_SCOPE_AGENT);
    SCHED();
    hstore(1, 2, swiglu8(acc[1][2]));
    STAMP(10);
    WAITV(0);
    if (lane == 0) __hip_atomic_store(fl + 32, ownn, __ATOMIC_RELAXED, __HIP_MEMORY_SCOPE_AGENT);
    if (w == 0) {
      hs_round(hv);
#pragma unroll 1
      for (int spin = 0; spin < (1 << 16) && !__all(lane >= 16 || ver); ++spin) {
        asm volatile("" ::: "memory");
        hv = hs_load();
        hs_round(hv);
      }
    }
    pn_out = pn;
    STAMP(11);
  }

  const int pi = (lane >> 1) & 15, pwf = lane & 1;
  const int pu0 = (pi < 6 ? 6 * pi : 36 + 5 * (pi - 6)) + 3 * pwf;
  const int pue = pwf ? (pi < 6 ? 6 * pi + 6 : 36 + 5 * (pi - 6) + 5) : pu0 + 3;
  const bool need = lane < 32 && pu0 < 22 * kq + 22 && pue > 22 * kq;
  GAS unsigned* pf = (GAS unsigned*)(cb + CB_FLAG) + ((j >> 3) * 2) * 32 + (lane & 31);
  auto poll = [&](GAS unsigned* f, unsigned want) {
#pragma unroll 1
    for (int spin = 0; spin < (1 << 16); ++spin) {
      const unsigned v = __hip_atomic_load(f, __ATOMIC_RELAXED, __HIP_MEMORY_SCOPE_AGENT);
      if (__all(!need || v == want)) break;
      __builtin_amdgcn_s_sleep(1);
    }
    __builtin_amdgcn_fence(__ATOMIC_ACQUIRE, "wavefront");
  };
  const int rot = OPT_HROT ? ((j & 7) * 10) >> 3 : 0;
  unsigned want;
  if (w == 0) {
    want = __shfl(pn_out, pi);
    if (lane < 16) ctrl[16 + lane] = pn_out;
    WAITL0();
    if (lane == 0) ctrl[1] = 1u;
  } else {
#pragma unroll 1
    while (ctrl[1] == 0u) __builtin_amdgcn_s_sleep(1);
    want = ctrl[16 + pi];
  }
  STAMP(12);
  poll(pf + mh * 32, want);
  STAMP(13);
  u32x4 hb[11][2];
  auto hload = [&](int i) {
    int sg;
    unsigned oob = 0;
    if (i == 0) sg = kq < 3 ? 11 * kq + 10 : 42;
    else {
      int rel = i - 1 + rot;
      rel = rel >= 10 ? rel - 10 : rel;
      sg = 11 * kq + rel;
      oob = (unsigned)(sg == 42) << 30;
    }
#pragma unroll
    for (int a = 0; a < 2; ++a) {
      const int mt = 4 * rb + 2 * mh + (a ^ (kq >> 1));
      const unsigned so = ((unsigned)(mt * NS2 + sg) * 1024) | oob;
      hb[i][a] = __builtin_bit_cast(u32x4, __builtin_amdgcn_raw_buffer_load_b128(rh, lane * 16, so, OPT_HAUX));
    }
  };
#pragma unroll
  for (int i = 0; i < OPT_HD; ++i) hload(i);
  SCHED();
#pragma unroll 1
  while (ctrl[0] < 4u) __builtin_amdgcn_s_sleep(1);
  asm volatile("" ::: "memory");
  STAMP(14);

  f32x4 acc2[4][2];
#pragma unroll
  for (int t = 0; t < 4; ++t)
#pragma unroll
    for (int a = 0; a < 2; ++a) acc2[t][a] = f32x4{0.f, 0.f, 0.f, 0.f};
#define MFMA16(A, B, C) __builtin_amdgcn_mfma_f32_16x16x32_bf16(__builtin_bit_cast(bf16x8, A), __builtin_bit_cast(bf16x8, B), C, 0, 0, 0)
  const int aoff2 = mk_aoff(opaque_lane());
  s16x8 af[2][4];
#pragma unroll
  for (int t = 0; t < 4; ++t) af[0][t] = a0[t];
#pragma unroll
  for (int i = 0; i < 11; ++i) {
    const int cur = i & 1, nxt = cur ^ 1;
    if (i + 1 < 11) {
      int rel = i + rot;
      rel = rel >= 10 ? rel - 10 : rel;
      int ridx = rel * 4 + kq;
      ridx = ridx > 38 ? 38 : ridx;
      int o = ridx - wrot;
      o = o < 0 ? o + 39 : o;
      const int roff = o < 9 ? (30 + o) * 4096 : (o < 19 ? (o + 11) * 4096 : (o - 19) * 4096);
#pragma unroll
      for (int t = 0; t < 4; ++t) af[nxt][t] = rdW(roff, aoff2, t);
    }
#pragma unroll
    for (int a = 0; a < 2; ++a)
#pragma unroll
      for (int t = 0; t < 4; ++t) acc2[t][a] = MFMA16(af[cur][t], hb[i][a], acc2[t][a]);
    if (i + 1 < 11) {
#pragma unroll
      for (int z = 0; z < 4; ++z) {
        __builtin_amdgcn_sched_group_barrier(0x100, 2, 1);
        __builtin_amdgcn_sched_group_barrier(0x8, 1, 1);
      }
      __builtin_amdgcn_sched_group_barrier(0x8, 4, 1);
    }
    SCHED();
    if (i + OPT_HD < 11) hload(i + OPT_HD);
    SCHED();
  }
  STAMP(15);
  BAR();
  LAS f32x4* red = (LAS f32x4*)smem;
#pragma unroll
  for (int g = 1; g < 4; ++g) {
    const int a = g >> 1, tp = g & 1;
#pragma unroll
    for (int t0 = 0; t0 < 2; ++t0) red[((((mh * 4 + (kq ^ g)) * 3 + (g - 1)) * 2 + t0) * 64) + lane] = acc2[2 * tp + t0][a];
  }
  WAITL0();
  BAR();
  f32x4 f0 = acc2[0][0], f1 = acc2[1][0];
#pragma unroll
  for (int g = 1; g < 4; ++g) {
    f0 += red[((((mh * 4 + kq) * 3 + (g - 1)) * 2 + 0) * 64) + lane];
    f1 += red[((((mh * 4 + kq) * 3 + (g - 1)) * 2 + 1) * 64) + lane];
  }
  {
    unsigned short* op = out + (size_t)(rb * 64 + 16 * (2 * mh + (kq >> 1)) + (lane & 15)) * D_ + n0 + 32 * (kq & 1) + 4 * q;
    *(u32x2*)op = u32x2{pack_bf16(f0[0], f0[1]), pack_bf16(f0[2], f0[3])};
    *(u32x2*)(op + 16) = u32x2{pack_bf16(f1[0], f1[1]), pack_bf16(f1[2], f1[3])};
  }
  STAMP(16);
}

extern "C" void kernel_launch(void* const* d_in, const int* in_sizes, int n_in, void* d_out, int out_size, void* d_ws,
                              size_t ws_size, hipStream_t stream) {
  const unsigned short* x = (const unsigned short*)d_in[0];
  const unsigned short* wg = (const unsigned short*)d_in[1];
  const unsigned short* wu = (const unsigned short*)d_in[2];
  const unsigned short* wd = (const unsigned short*)d_in[3];
  (void)hipFuncSetAttribute((const void*)ffn_fused, hipFuncAttributeMaxDynamicSharedMemorySize, LDS_BYTES);
  ffn_fused<<<256, 512, LDS_BYTES, stream>>>(x, wg, wu, wd, (unsigned char*)d_ws, (unsigned short*)d_out);
}
```

```cpp
#include <hip/hip_runtime.h>
#include <hip/hip_bf16.h>

typedef short s16x8 __attribute__((ext_vector_type(8)));
typedef short s16x4 __attribute__((ext_vector_type(4)));
typedef __bf16 bf16x8 __attribute__((ext_vector_type(8)));
typedef float f32x2 __attribute__((ext_vector_type(2)));
typedef float f32x4 __attribute__((ext_vector_type(4)));
typedef float f32x16 __attribute__((ext_vector_type(16)));
typedef unsigned u32x4 __attribute__((ext_vector_type(4)));
typedef unsigned u32x2 __attribute__((ext_vector_type(2)));
#define LAS __attribute__((address_space(3)))
#define GAS __attribute__((address_space(1)))

constexpr int M_ = 2048, D_ = 512, F_ = 1376;
constexpr int NS2 = F_ / 32;
constexpr int SLOT = 40960, STG_X = 16384, STG_W = 12288;
constexpr int LDS_BYTES = 163840;
constexpr int LDS_CTRL = 159744;
constexpr unsigned CB_PANEL = 8192, CB_N = 0, CB_FLAG = 512, CB_E = 2048;
constexpr unsigned H_OFF = 16 * CB_PANEL;
constexpr unsigned H_BYTES = M_ * F_ * 2;
constexpr unsigned OOB = 0x40000000u;
constexpr int AUX_SC1 = 16;

constexpr int ROT_MASK = 6;
constexpr int WD_E1 = 15;
constexpr int WD_E2 = 19;
constexpr int H_DIST = 3;

__device__ __forceinline__ unsigned umin_(unsigned a, unsigned b) { return a < b ? a : b; }
__device__ __forceinline__ unsigned pack_bf16(float lo, float hi) {
  typedef __bf16 bf16x2 __attribute__((ext_vector_type(2)));
  f32x2 v = {lo, hi};
  bf16x2 b = __builtin_convertvector(v, bf16x2);
  return __builtin_bit_cast(unsigned, b);
}

#define GLDS(M0V, VOFF, RS, SOFF)                                                                    \
  asm volatile("s_mov_b32 m0, %0\n\ts_nop 0\n\tbuffer_load_dwordx4 %1, %2, %3 offen lds" ::"s"(M0V), \
               "v"(VOFF), "s"(RS), "s"(SOFF)                                                         \
               : "memory")
#define WAITV(n) asm volatile("s_waitcnt vmcnt(" #n ")" ::: "memory")
#define WAITL0() asm volatile("s_waitcnt lgkmcnt(0)" ::: "memory")
#define BAR()                              \
  do {                                     \
    asm volatile("" ::: "memory");         \
    __builtin_amdgcn_s_barrier();          \
    asm volatile("" ::: "memory");         \
  } while (0)
#define SCHED() __builtin_amdgcn_sched_barrier(0)
#define EPI_SCHED()                                         \
  do {                                                      \
    _Pragma("unroll") for (int z_ = 0; z_ < 4; ++z_) {      \
      __builtin_amdgcn_sched_group_barrier(0x8, 1, 2);      \
      __builtin_amdgcn_sched_group_barrier(0x402, 6, 2);    \
    }                                                       \
    __builtin_amdgcn_sched_barrier(0);                      \
  } while (0)

__device__ __forceinline__ u32x4 swiglu8(const f32x16& c) {
  u32x4 o;
#pragma unroll
  for (int p = 0; p < 4; ++p) {
    f32x2 g = {c[2 * p], c[2 * p + 1]};
    f32x2 u = {c[8 + 2 * p], c[8 + 2 * p + 1]};
    f32x2 t = g * -1.4426950408889634f;
    f32x2 e = {__builtin_amdgcn_exp2f(t[0]), __builtin_amdgcn_exp2f(t[1])};
    e = e + 1.0f;
    f32x2 r = {__builtin_amdgcn_rcpf(e[0]), __builtin_amdgcn_rcpf(e[1])};
    f32x2 h = (g * u) * r;
    o[p] = pack_bf16(h[0], h[1]);
  }
  return o;
}

__global__ __launch_bounds__(512, 2) void ffn_fused(const unsigned short* __restrict__ x,
                                                     const unsigned short* __restrict__ wg,
                                                     const unsigned short* __restrict__ wu,
                                                     const unsigned short* __restrict__ wd,
                                                     unsigned char* __restrict__ ws,
                                                     unsigned short* __restrict__ out) {
  extern __shared__ __attribute__((aligned(16))) unsigned char smem[];
  asm volatile("" ::"s"(x), "s"(wg), "s"(wu), "s"(wd), "s"(ws), "s"(out));
  const int tid = threadIdx.x, lane = tid & 63;
  const int w = __builtin_amdgcn_readfirstlane(tid >> 6);
  const int b = blockIdx.x;
  const int xcd = b & 7, idx = b >> 3;
  const int pl = idx & 3, jl = idx >> 2;
  const int p = 4 * (xcd >> 1) + pl;
  const int j = 8 * (xcd & 1) + jl;
  const int u0 = j < 6 ? 6 * j : 36 + 5 * (j - 6);
  const int nu = j < 6 ? 6 : 5;
  const int m0 = p * 128;
  const int rho = (jl + 2 * pl) & ROT_MASK;
  const int wrot = (pl * 39) >> 2;
  const unsigned ldsb = (unsigned)(size_t)(LAS unsigned char*)smem;
  const LAS unsigned char* L = (const LAS unsigned char*)smem;

  __amdgpu_buffer_rsrc_t rh = __builtin_amdgcn_make_buffer_rsrc((void*)(ws + H_OFF), 0, H_BYTES, 0x00020000);
  GAS unsigned char* cb = (GAS unsigned char*)ws + p * CB_PANEL;

  const int kq = w & 3;
  const int mh = w >> 2;
  const int rb = 2 * p + (j >> 3);
  const int n0 = (j & 7) * 64;
  const int q = lane >> 4;

  auto mk_aoff = [&](int lane_) -> int {
    const int q_ = lane_ >> 4, qp = (lane_ >> 2) & 3, pp = lane_ & 3;
    const int row = 16 * (q_ >> 1) + 4 * (q_ & 1) + qp;
    const int g = 2 * (q_ & 1) + (qp >> 1);
    return row * 128 + (((2 * (kq & 1)) ^ g) * 32) + pp * 8;
  };
  auto rdW = [&](int roff, int aoff, int t) -> s16x8 {
    const LAS unsigned char* pp = L + roff + (aoff ^ (t << 5));
    s16x4 lo = __builtin_amdgcn_ds_read_tr16_b64_v4i16((LAS s16x4*)pp);
    s16x4 hi = __builtin_amdgcn_ds_read_tr16_b64_v4i16((LAS s16x4*)(pp + 1024));
    return __builtin_shufflevector(lo, hi, 0, 1, 2, 3, 4, 5, 6, 7);
  };
  auto opaque_lane = [&]() -> int { int l = tid & 63; asm volatile("" : "+v"(l)); return l; };
  s16x8 a0[4];
  volatile LAS unsigned* ctrl = (volatile LAS unsigned*)(smem + LDS_CTRL);

  if (__builtin_expect(w >= 4, 1)) {
    const int lw = w - 4;
    __amdgpu_buffer_rsrc_t rx = __builtin_amdgcn_make_buffer_rsrc((void*)x, 0, M_ * D_ * 2, 0x00020000);
    __amdgpu_buffer_rsrc_t rg = __builtin_amdgcn_make_buffer_rsrc((void*)wg, 0, D_ * F_ * 2, 0x00020000);
    __amdgpu_buffer_rsrc_t ru = __builtin_amdgcn_make_buffer_rsrc((void*)wu, 0, D_ * F_ * 2, 0x00020000);
    __amdgpu_buffer_rsrc_t rd = __builtin_amdgcn_make_buffer_rsrc((void*)wd, 0, F_ * D_ * 2, 0x00020000);
    const int xoE = (lane >> 3) * 1024 + (((lane & 7) ^ ((lane >> 4) & 3)) * 16), xoO = xoE ^ 64;
    const unsigned xs0 = (unsigned)(m0 + 32 * lw) * 1024;
    const unsigned m0x = ldsb + lw * 4096, m0g = ldsb + STG_X + lw * 3072, m0u = m0g + STG_W;
    {
      const unsigned sx = xs0 + rho * 128;
      GLDS(m0x + SLOT, xoE, rx, sx);
      GLDS(m0x + SLOT + 1024, xoO, rx, sx + 8192);
      GLDS(m0x + SLOT + 2048, xoE, rx, sx + 16384);
      GLDS(m0x + SLOT + 3072, xoO, rx, sx + 24576);
    }
    int goff[3], uoff[3];
    const int ud = (lane & 2) ? -32 : 32;
#pragma unroll
    for (int i = 0; i < 3; ++i) {
      const int t = 64 * i + lane;
      const int k = (int)(__umul24(t, 171) >> 11);
      const int cc = t - 12 * k;
      const int g0 = k * 2560 + 16 * t;
      goff[i] = (nu == 5 && cc >= 10) ? (int)OOB : g0;
      uoff[i] = (nu == 5 && (cc >> 1) == 4) ? (int)OOB : g0 + ud;
    }
    const unsigned ws0 = (unsigned)(16 * lw) * (F_ * 2) + u0 * 32;
    auto issue = [&](int pos) {
      const unsigned sb = ((pos + 1) % 3) * SLOT;
      const int st = (pos + rho) & 7;
      const unsigned sx = xs0 + st * 128, sw = ws0 + st * (64 * F_ * 2);
      if (pos > 0) {
        GLDS(m0x + sb, xoE, rx, sx);
        GLDS(m0x + sb + 1024, xoO, rx, sx + 8192);
        GLDS(m0x + sb + 2048, xoE, rx, sx + 16384);
        GLDS(m0x + sb + 3072, xoO, rx, sx + 24576);
      }
#pragma unroll
      for (int i = 0; i < 3; ++i) GLDS(m0g + sb + i * 1024, goff[i], rg, sw);
#pragma unroll
      for (int i = 0; i < 3; ++i) GLDS(m0u + sb + i * 1024, uoff[i], ru, sw);
    };
    const int drow = lane >> 3;
    const int doff = (8 * lw + drow) * (D_ * 2) + n0 * 2 + ((((lane & 7) >> 1) ^ ((drow >> 1) & 3)) * 32) + (lane & 1) * 16;
    auto issue_wd = [&](int o) {
      const unsigned r0 = o + wrot, r = umin_(r0, r0 - 39u);
      const unsigned u0_ = o + 30, unit = umin_(u0_, u0_ - 39u);
      GLDS(ldsb + unit * 4096 + lw * 1024, doff, rd, (r + ((r * 205u) >> 11)) * (32 * D_ * 2));
    };
    issue(0);
    issue(1);
    WAITV(10);
    BAR();
    issue(2);
    WAITV(10);
    BAR();
#pragma unroll
    for (int r = 0; r < 4; ++r) GLDS(ldsb + (30 + r) * 4096 + lw * 1024, doff, rd, (unsigned)(r < 3 ? 11 * r + 10 : 42) * (32 * D_ * 2));
    issue(3);
    WAITV(14);
    BAR();
    issue(4);
    WAITV(10);
    BAR();
    {
      const int ao = mk_aoff(opaque_lane());
#pragma unroll
      for (int t = 0; t < 4; ++t) a0[t] = rdW((30 + kq) * 4096, ao, t);
    }
    issue(5);
    WAITV(10);
    WAITL0();
    BAR();
    issue(6);
#pragma unroll
    for (int r = 0; r < 3; ++r) issue_wd(r);
    WAITV(13);
    BAR();
    issue(7);
#pragma unroll
    for (int r = 3; r < 6; ++r) issue_wd(r);
    WAITV(16);
    BAR();
#pragma unroll
    for (int r = 6; r < WD_E1; ++r) issue_wd(r);
    asm volatile("s_waitcnt vmcnt(%0)" ::"i"(WD_E1 - 3) : "memory");
    BAR();
#pragma unroll 1
    for (int o = WD_E1; o < WD_E2; ++o) issue_wd(o);
#pragma unroll 1
    while (ctrl[3] < 4u) __builtin_amdgcn_s_sleep(1);
    asm volatile("" ::: "memory");
#pragma unroll 2
    for (int o = WD_E2; o < 39; ++o) issue_wd(o);
    WAITV(0);
    if (lane == 0) __hip_atomic_fetch_add((LAS unsigned*)ctrl, 1u, __ATOMIC_RELAXED, __HIP_MEMORY_SCOPE_WORKGROUP);
  } else {
    const int wm = w >> 1, wf = w & 1;
    const int hh = lane >> 5;
    const int xsw = ((lane & 31) >> 1) & 7;
    const int xrow = (wm * 64 + (lane & 31)) * 128;
    int xk[4];
#pragma unroll
    for (int ks = 0; ks < 4; ++ks) xk[ks] = xrow + (((2 * ks + hh) ^ xsw) * 16);
    const int bsel = (lane >> 4) & 1;
    const int wbase = STG_X + bsel * STG_W + (8 * hh + ((lane >> 2) & 3)) * 192 + (lane & 3) * 8;
    int wa[3];
#pragma unroll
    for (int u = 0; u < 3; ++u) wa[u] = wbase + ((wf * 3 + u) ^ bsel) * 32;

    f32x16 acc[2][3];
#pragma unroll
    for (int a = 0; a < 2; ++a)
#pragma unroll
      for (int u = 0; u < 3; ++u)
#pragma unroll
        for (int r = 0; r < 16; ++r) acc[a][u][r] = 0.f;

    auto rdA = [&](int sb, int ks, int u) -> s16x8 {
      const LAS unsigned char* pp = L + sb + wa[u] + ks * (16 * 192);
      s16x4 lo = __builtin_amdgcn_ds_read_tr16_b64_v4i16((LAS s16x4*)pp);
      s16x4 hi = __builtin_amdgcn_ds_read_tr16_b64_v4i16((LAS s16x4*)(pp + 4 * 192));
      return __builtin_shufflevector(lo, hi, 0, 1, 2, 3, 4, 5, 6, 7);
    };
    auto rdB = [&](int sb, int ks, int a) -> s16x8 { return *(const LAS s16x8*)(L + sb + xk[ks] + a * 4096); };
#define MFMA32(A, B, C) __builtin_amdgcn_mfma_f32_32x32x16_bf16(__builtin_bit_cast(bf16x8, A), __builtin_bit_cast(bf16x8, B), C, 0, 0, 0)

    __amdgpu_buffer_rsrc_t rc = __builtin_amdgcn_make_buffer_rsrc((void*)cb, 0, CB_PANEL, 0x00020000);
    int goffs = (int)OOB, eoffs = (int)OOB, noffs = (int)OOB;
    if (w == 0) {
      if (lane < 16) { goffs = CB_E + j * 128 + lane * 8; eoffs = CB_E + lane * 128 + j * 8; }
      else if (lane < 32) goffs = CB_N + (lane - 16) * 8;
      else if (lane < 36) goffs = CB_FLAG + (lane - 32) * 128 + j * 8;
      if (lane == 0) noffs = CB_N + j * 8;
    }
    unsigned myn = 0, pn = 0;
    bool ver = false;
    auto hs_load = [&]() -> u32x2 { return __builtin_bit_cast(u32x2, __builtin_amdgcn_raw_buffer_load_b64(rc, goffs, 0, AUX_SC1)); };
    auto hs_round = [&](const u32x2& hv) {
      const unsigned nrow = __shfl(hv[0], (lane & 15) + 16);
      const bool nv_ = !ver && hv[0] == myn;
      pn = nv_ ? hv[1] : pn;
      ver = ver || nv_;
      const u32x2 val = {ver ? pn : nrow, myn};
      __builtin_amdgcn_raw_buffer_store_b64(val, rc, eoffs, 0, AUX_SC1);
    };
    u32x2 hv = hs_load();

    s16x8 fa[2][3], fb[2][2];
    unsigned ownn = 0;
    if (tid >= 64 && tid < 68) ctrl[tid - 64] = 0u;
    WAITL0();
    BAR();
#pragma unroll
    for (int u = 0; u < 3; ++u) fa[0][u] = rdA(SLOT, 0, u);
#pragma unroll
    for (int a = 0; a < 2; ++a) fb[0][a] = rdB(SLOT, 0, a);
#pragma unroll
    for (int g = 0; g < 28; ++g) {
      const int pos = g >> 2, ks = g & 3;
      if (ks == 3) {
        SCHED();
        if (pos == 5)
          ownn = __hip_atomic_load((GAS unsigned*)(cb + CB_N + j * 8), __ATOMIC_RELAXED, __HIP_MEMORY_SCOPE_AGENT);
        if (pos == 0) {
          asm volatile("" : "+v"(hv));
          unsigned c = __shfl(hv[0], 16 + j);
          const bool mine = lane < 16 || (lane >= 32 && lane < 36);
#pragma unroll 1
          do { ++c; } while (__any(mine && (hv[0] == c || hv[1] == c)));
          myn = c;
          __builtin_amdgcn_raw_buffer_store_b64(u32x2{myn, myn}, rc, noffs, 0, AUX_SC1);
        }
        if (pos == 4) {
          asm volatile("" : "+v"(hv));
          hs_round(hv);
        }
        if (pos == 2 || pos == 6) hv = hs_load();
        if (pos == 3) {
          const int ao = mk_aoff(opaque_lane());
#pragma unroll
          for (int t = 0; t < 4; ++t) a0[t] = rdW((30 + kq) * 4096, ao, t);
        }
        WAITL0();
        BAR();
      }
      SCHED();
      const int cur = g & 1, nxt = cur ^ 1;
      if (g + 1 < 28) {
        const int npos = (g + 1) >> 2, nks = (g + 1) & 3, sb = ((npos + 1) % 3) * SLOT;
        fb[nxt][0] = rdB(sb, nks, 0);
#pragma unroll
        for (int u = 0; u < 3; ++u) fa[nxt][u] = rdA(sb, nks, u);
        fb[nxt][1] = rdB(sb, nks, 1);
#pragma unroll
        for (int a = 0; a < 2; ++a)
#pragma unroll
          for (int u = 0; u < 3; ++u) acc[a][u] = MFMA32(fa[cur][u], fb[cur][a], acc[a][u]);
#pragma unroll
        for (int i = 0; i < 4; ++i) {
          __builtin_amdgcn_sched_group_barrier(0x8, 1, 0);
          __builtin_amdgcn_sched_group_barrier(0x100, 2, 0);
        }
        __builtin_amdgcn_sched_group_barrier(0x8, 2, 0);
      }
    }
    s16x8 la[3][4], lb[2][4];
    {
      constexpr int sb = ((7 + 1) % 3) * SLOT;
#pragma unroll
      for (int ks = 0; ks < 4; ++ks) {
        la[0][ks] = rdA(sb, ks, 0);
        lb[0][ks] = rdB(sb, ks, 0);
      }
#pragma unroll
      for (int a = 0; a < 2; ++a)
#pragma unroll
        for (int u = 0; u < 3; ++u) acc[a][u] = MFMA32(fa[1][u], fb[1][a], acc[a][u]);
#pragma unroll
      for (int i = 0; i < 6; ++i) {
        __builtin_amdgcn_sched_group_barrier(0x8, 1, 0);
        __builtin_amdgcn_sched_group_barrier(0x100, 2, 0);
      }
      SCHED();
#pragma unroll
      for (int ks = 0; ks < 4; ++ks) la[1][ks] = rdA(sb, ks, 1);
#pragma unroll
      for (int ks = 0; ks < 4; ++ks) acc[0][0] = MFMA32(la[0][ks], lb[0][ks], acc[0][0]);
#pragma unroll
      for (int i = 0; i < 4; ++i) {
        __builtin_amdgcn_sched_group_barrier(0x100, 2, 0);
        __builtin_amdgcn_sched_group_barrier(0x8, 1, 0);
      }
      SCHED();
#pragma unroll
      for (int ks = 0; ks < 4; ++ks) la[2][ks] = rdA(sb, ks, 2);
#pragma unroll
      for (int ks = 0; ks < 4; ++ks) lb[1][ks] = rdB(sb, ks, 1);
    }
    const int ugb = u0 + wf * 3;
    const int nuw = wf ? nu - 3 : 3;
    auto hstore = [&](int a, int u, u32x4 v) {
      const int m = m0 + wm * 64 + a * 32 + (lane & 31);
      const int ug = ugb + u;
      const int voff = ((m >> 4) * NS2 * 64 + (m & 15) + 16 * hh) * 16;
      const unsigned soff = u < nuw ? (unsigned)((ug >> 1) * 1024 + (ug & 1) * 512) : OOB;
      __builtin_amdgcn_raw_buffer_store_b128(v, rh, voff + (int)soff, 0, AUX_SC1);
    };
#pragma unroll
    for (int ks = 0; ks < 4; ++ks) acc[0][1] = MFMA32(la[1][ks], lb[0][ks], acc[0][1]);
    hstore(0, 0, swiglu8(acc[0][0]));
    EPI_SCHED();
    WAITL0();
    SCHED();
#pragma unroll
    for (int ks = 0; ks < 4; ++ks) acc[0][2] = MFMA32(la[2][ks], lb[0][ks], acc[0][2]);
    hstore(0, 1, swiglu8(acc[0][1]));
    EPI_SCHED();
#pragma unroll
    for (int ks = 0; ks < 4; ++ks) acc[1][0] = MFMA32(la[0][ks], lb[1][ks], acc[1][0]);
    hstore(0, 2, swiglu8(acc[0][2]));
    EPI_SCHED();
#pragma unroll
    for (int ks = 0; ks < 4; ++ks) acc[1][1] = MFMA32(la[1][ks], lb[1][ks], acc[1][1]);
    hstore(1, 0, swiglu8(acc[1][0]));
    EPI_SCHED();
#pragma unroll
    for (int ks = 0; ks < 4; ++ks) acc[1][2] = MFMA32(la[2][ks], lb[1][ks], acc[1][2]);
    hstore(1, 1, swiglu8(acc[1][1]));
    EPI_SCHED();
    hstore(1, 2, swiglu8(acc[1][2]));
    if (lane == 0) __hip_atomic_fetch_add((LAS unsigned*)ctrl + 3, 1u, __ATOMIC_RELAXED, __HIP_MEMORY_SCOPE_WORKGROUP);
    GAS unsigned* fl = (GAS unsigned*)(cb + CB_FLAG) + (wm * 2) * 32 + j * 2 + wf;
    WAITV(3);
    if (lane == 0) __hip_atomic_store(fl, ownn, __ATOMIC_RELAXED, __HIP_MEMORY_SCOPE_AGENT);
    bool pub = false;
    if (w == 0) {
      const bool nv_ = !ver && hv[0] == myn;
      pn = nv_ ? hv[1] : pn;
      ver = ver || nv_;
      pub = __all(lane >= 16 || ver);
      if (pub) {
        if (lane < 16) ctrl[16 + lane] = pn;
        WAITL0();
        if (lane == 0) ctrl[1] = 1u;
      }
    }
    WAITV(1);
    if (lane == 0) __hip_atomic_store(fl + 32, ownn, __ATOMIC_RELAXED, __HIP_MEMORY_SCOPE_AGENT);
    if (w == 0) {
      hs_round(hv);
      if (!pub) {
#pragma unroll 1
        for (int spin = 0; spin < (1 << 16) && !__all(lane >= 16 || ver); ++spin) {
          asm volatile("" ::: "memory");
          hv = hs_load();
          hs_round(hv);
        }
        if (lane < 16) ctrl[16 + lane] = pn;
        WAITL0();
        if (lane == 0) ctrl[1] = 1u;
      }
    }
  }

  const int pi = (lane >> 1) & 15, pwf = lane & 1;
  const int pu0 = (pi < 6 ? 6 * pi : 36 + 5 * (pi - 6)) + 3 * pwf;
  const int pue = pwf ? (pi < 6 ? 6 * pi + 6 : 36 + 5 * (pi - 6) + 5) : pu0 + 3;
  const bool need = lane < 32 && pu0 < 22 * kq + 22 && pue > 22 * kq;
  GAS unsigned* pf = (GAS unsigned*)(cb + CB_FLAG) + ((j >> 3) * 2) * 32 + (lane & 31);
  auto poll = [&](GAS unsigned* f, unsigned want) {
#pragma unroll 1
    for (int spin = 0; spin < (1 << 16); ++spin) {
      const unsigned v = __hip_atomic_load(f, __ATOMIC_RELAXED, __HIP_MEMORY_SCOPE_AGENT);
      if (__all(!need || v == want)) break;
      __builtin_amdgcn_s_sleep(1);
    }
    __builtin_amdgcn_fence(__ATOMIC_ACQUIRE, "wavefront");
  };
  const int rot = ((j & 7) * 10) >> 3;
#pragma unroll 1
  while (ctrl[1] == 0u) __builtin_amdgcn_s_sleep(1);
  const unsigned want = ctrl[16 + pi];
  if (mh) __builtin_amdgcn_s_setprio(2);
  poll(pf + mh * 32, want);
  u32x4 hb[11][2];
  auto hload = [&](int i) {
    int sg;
    unsigned oob = 0;
    if (i == 0) sg = kq < 3 ? 11 * kq + 10 : 42;
    else {
      const unsigned r0 = i - 1 + rot;
      sg = 11 * kq + (int)umin_(r0, r0 - 10u);
      oob = (unsigned)(sg == 42) << 30;
    }
#pragma unroll
    for (int a = 0; a < 2; ++a) {
      const int mt = 4 * rb + 2 * mh + (a ^ (kq >> 1));
      const unsigned so = ((unsigned)(mt * NS2 + sg) * 1024) | oob;
      hb[i][a] = __builtin_bit_cast(u32x4, __builtin_amdgcn_raw_buffer_load_b128(rh, lane * 16, so, AUX_SC1));
    }
  };
#pragma unroll
  for (int i = 0; i < H_DIST; ++i) hload(i);
  SCHED();
#pragma unroll 1
  while (ctrl[0] < 4u) __builtin_amdgcn_s_sleep(1);
  asm volatile("" ::: "memory");

  f32x4 acc2[4][2];
#pragma unroll
  for (int t = 0; t < 4; ++t)
#pragma unroll
    for (int a = 0; a < 2; ++a) acc2[t][a] = f32x4{0.f, 0.f, 0.f, 0.f};
#define MFMA16(A, B, C) __builtin_amdgcn_mfma_f32_16x16x32_bf16(__builtin_bit_cast(bf16x8, A), __builtin_bit_cast(bf16x8, B), C, 0, 0, 0)
  const int aoff2 = mk_aoff(opaque_lane());
  s16x8 af[2][4];
#pragma unroll
  for (int t = 0; t < 4; ++t) af[0][t] = a0[t];
#pragma unroll
  for (int i = 0; i < 11; ++i) {
    const int cur = i & 1, nxt = cur ^ 1;
    if (i + 1 < 11) {
      const unsigned r0 = i + rot, rel = umin_(r0, r0 - 10u);
      const unsigned v0 = umin_(10 * kq + rel, 38u) + (30 - wrot);
      const int roff = (int)umin_(v0, v0 - 39u) * 4096;
#pragma unroll
      for (int t = 0; t < 4; ++t) af[nxt][t] = rdW(roff, aoff2, t);
    }
#pragma unroll
    for (int a = 0; a < 2; ++a)
#pragma unroll
      for (int t = 0; t < 4; ++t) acc2[t][a] = MFMA16(af[cur][t], hb[i][a], acc2[t][a]);
    if (i + 1 < 11) {
#pragma unroll
      for (int z = 0; z < 4; ++z) {
        __builtin_amdgcn_sched_group_barrier(0x100, 2, 1);
        __builtin_amdgcn_sched_group_barrier(0x8, 1, 1);
      }
      __builtin_amdgcn_sched_group_barrier(0x8, 4, 1);
    }
    SCHED();
    if (i + H_DIST < 11) hload(i + H_DIST);
    SCHED();
  }
  BAR();
  LAS f32x4* red = (LAS f32x4*)smem;
#pragma unroll
  for (int g = 1; g < 4; ++g) {
    const int a = g >> 1, tp = g & 1;
#pragma unroll
    for (int t0 = 0; t0 < 2; ++t0) red[((((mh * 4 + (kq ^ g)) * 3 + (g - 1)) * 2 + t0) * 64) + lane] = acc2[2 * tp + t0][a];
  }
  WAITL0();
  BAR();
  f32x4 f0 = acc2[0][0], f1 = acc2[1][0];
#pragma unroll
  for (int g = 1; g < 4; ++g) {
    f0 += red[((((mh * 4 + kq) * 3 + (g - 1)) * 2 + 0) * 64) + lane];
    f1 += red[((((mh * 4 + kq) * 3 + (g - 1)) * 2 + 1) * 64) + lane];
  }
  {
    __amdgpu_buffer_rsrc_t ro = __builtin_amdgcn_make_buffer_rsrc((void*)out, 0, M_ * D_ * 2, 0x00020000);
    const int oo = ((rb * 64 + 16 * (2 * mh + (kq >> 1)) + (lane & 15)) * D_ + n0 + 32 * (kq & 1) + 4 * q) * 2;
    unsigned x0 = pack_bf16(f0[0], f0[1]), x1 = pack_bf16(f0[2], f0[3]), y0 = pack_bf16(f1[0], f1[1]), y1 = pack_bf16(f1[2], f1[3]);
    auto r0 = __builtin_amdgcn_permlane16_swap(x0, y0, false, false);
    auto r1 = __builtin_amdgcn_permlane16_swap(x1, y1, false, false);
    const int o2 = oo + ((q & 1) ? (16 - 4) * 2 : 0);
    __builtin_amdgcn_raw_buffer_store_b128(u32x4{r0[0], r1[0], r0[1], r1[1]}, ro, o2, 0, AUX_SC1);
  }
}

extern "C" void kernel_launch(void* const* d_in, const int* in_sizes, int n_in, void* d_out, int out_size, void* d_ws,
                              size_t ws_size, hipStream_t stream) {
  const unsigned short* x = (const unsigned short*)d_in[0];
  const unsigned short* wg = (const unsigned short*)d_in[1];
  const unsigned short* wu = (const unsigned short*)d_in[2];
  const unsigned short* wd = (const unsigned short*)d_in[3];
  (void)hipFuncSetAttribute((const void*)ffn_fused, hipFuncAttributeMaxDynamicSharedMemorySize, LDS_BYTES);
  ffn_fused<<<256, 512, LDS_BYTES, stream>>>(x, wg, wu, wd, (unsigned char*)d_ws, (unsigned short*)d_out);
}
```
